```python
import torch

def _softmax(x, axis=-1):
    return torch.softmax(x, dim=axis)
def _gelu(x):
    return torch.nn.functional.gelu(x, approximate="tanh")
def _silu(x):
    return torch.nn.functional.silu(x)
def _sigmoid(x):
    return torch.sigmoid(x)
def _layernorm(x, axis=-1, eps=1e-5):
    m = x.mean(dim=axis, keepdim=True)
    v = x.var(dim=axis, keepdim=True, unbiased=False)
    return (x - m) / torch.sqrt(v + eps)
def _scatter_add(tgt, idx, src, axis=0):
    return tgt.clone().index_add_(axis, idx, src)
def _segment_sum(src, seg, n):
    out = torch.zeros((n,) + src.shape[1:], dtype=src.dtype)
    return out.index_add_(0, seg, src)
def _pad(x, pw):
    flat = tuple(v for pair in reversed(pw) for v in pair)
    return torch.nn.functional.pad(x, flat)

kernel_name = 'standalone__attention_interact_autoint__recsys'

def setup_inputs():

    _w = (lambda a: (lambda t: t.to(torch.bfloat16) if t.is_floating_point() else t)(torch.as_tensor(a)))
    B, F = 4096, 26
    D = max(2, (128 // 2) * 2)
    H = 2
    Dh = D // H
    return {
        "e": _w((lambda *s: torch.randn(*s, dtype=torch.bfloat16) * 0.05)(B, F, D).to(float)),
        "wq": _w((lambda *s: torch.randn(*s, dtype=torch.bfloat16) * 1.6)(H, D, Dh).to(float)),
        "wk": _w((lambda *s: torch.randn(*s, dtype=torch.bfloat16) * 1.6)(H, D, Dh).to(float)),
        "wv": _w((lambda *s: torch.randn(*s, dtype=torch.bfloat16) * 0.05)(H, D, Dh).to(float)),
        "wres": _w((lambda *s: torch.randn(*s, dtype=torch.bfloat16) * 0.05)(D, D).to(float)),
    }

def reference(e, wq, wk, wv, wres):
    H, D, Dh = wq.shape[0], wq.shape[1], wq.shape[2]
    B, F = e.shape[0], e.shape[1]
    q = torch.einsum('bfd,hdk->bhfk', e, wq)
    k = torch.einsum('bfd,hdk->bhfk', e, wk)
    v = torch.einsum('bfd,hdk->bhfk', e, wv)
    scale = float(Dh) ** -0.5
    scores = torch.einsum('bhfk,bhgk->bhfg', q, k) * scale
    attn = _softmax(scores, axis=-1)
    attn = (lambda x, dt: x.to(dt))(attn, v.dtype)
    ctx = torch.einsum('bhfg,bhgk->bhfk', attn, v)
    ctx = (lambda x, *d: x.permute(*d))(ctx, 0, 2, 1, 3).reshape(B, F, H * Dh)
    res = torch.einsum('bfd,de->bfe', e, wres)
    out = ctx + res
    mask = (lambda x, dt: x.to(dt))(out > 0, out.dtype)
    return out * mask
```

```cpp
#include <hip/hip_runtime.h>
#include <hip/hip_bf16.h>

typedef __bf16 bf16x8 __attribute__((ext_vector_type(8)));
typedef __bf16 bf16x2 __attribute__((ext_vector_type(2)));
typedef float f32x16 __attribute__((ext_vector_type(16)));
typedef float f32x2 __attribute__((ext_vector_type(2)));
typedef unsigned u32x4 __attribute__((ext_vector_type(4)));
typedef unsigned u32x2 __attribute__((ext_vector_type(2)));
typedef short v4i16_t __attribute__((ext_vector_type(4)));

#define DI __device__ __forceinline__
#define MFMA(a, b, c) __builtin_amdgcn_mfma_f32_32x32x16_bf16((a), (b), (c), 0, 0, 0)

constexpr int F_ = 26, D_ = 128, ROWB = D_ * 2, BATCHB = F_ * ROWB;

DI unsigned pk2(float lo, float hi) {
  bf16x2 p = __builtin_convertvector(f32x2{lo, hi}, bf16x2);
  return __builtin_bit_cast(unsigned, p);
}
DI bf16x8 pack8(const f32x16& x, int u) {
  u32x4 p;
  p[0] = pk2(x[8 * u + 0], x[8 * u + 1]);
  p[1] = pk2(x[8 * u + 2], x[8 * u + 3]);
  p[2] = pk2(x[8 * u + 4], x[8 * u + 5]);
  p[3] = pk2(x[8 * u + 6], x[8 * u + 7]);
  return __builtin_bit_cast(bf16x8, p);
}
DI float swapmax(float m) {
  auto rr = __builtin_amdgcn_permlane32_swap(__float_as_uint(m), __float_as_uint(m), false, false);
  return __builtin_fmaxf(__uint_as_float(rr[0]), __uint_as_float(rr[1]));
}
DI float swapadd(float m) {
  auto rr = __builtin_amdgcn_permlane32_swap(__float_as_uint(m), __float_as_uint(m), false, false);
  return __uint_as_float(rr[0]) + __uint_as_float(rr[1]);
}

__global__ __launch_bounds__(256, 1) void autoint_kernel(const __hip_bfloat16* __restrict__ e,
                                                          const __hip_bfloat16* __restrict__ wq,
                                                          const __hip_bfloat16* __restrict__ wk,
                                                          const __hip_bfloat16* __restrict__ wv,
                                                          const __hip_bfloat16* __restrict__ wres,
                                                          __hip_bfloat16* __restrict__ out, int B) {
  __shared__ __attribute__((aligned(16))) char lds[4 * 128 * 128];
  const int tid = threadIdx.x, lane = tid & 63, r = lane & 31, h = lane >> 5;
  const int wave = __builtin_amdgcn_readfirstlane(tid >> 6);
  const int bid = blockIdx.x;
  const int xcd = bid & 7, slot = bid >> 3, head = slot & 1;
  const int nchunk = gridDim.x >> 1;
  const int chunk = (slot >> 1) + (nchunk >> 3) * xcd;
  const int gw = chunk * 4 + wave, nw = nchunk * 4;

  const unsigned ebytes = (unsigned)B * BATCHB;
  __amdgpu_buffer_rsrc_t ers = __builtin_amdgcn_make_buffer_rsrc((void*)e, 0, ebytes, 0x00020000);
  __amdgpu_buffer_rsrc_t ors = __builtin_amdgcn_make_buffer_rsrc((void*)out, 0, ebytes, 0x00020000);
  const unsigned BAD = 0x80000000u;
  const unsigned lane_in = r < F_ ? (unsigned)(r * ROWB + h * 16) : BAD;
  const unsigned lane_out = r < F_ ? (unsigned)(r * ROWB + head * 128 + h * 16) : BAD;

  {
    const char* srcs[4] = {(const char*)wq + head * 16384, (const char*)wk + head * 16384,
                           (const char*)wv + head * 16384, (const char*)wres + head * 128};
#pragma unroll
    for (int m = 0; m < 4; ++m) {
#pragma unroll
      for (int i = 0; i < 4; ++i) {
        const int c = i * 256 + tid;
        const int d = c >> 3, cc = c & 7;
        const char* g = srcs[m] + (m < 3 ? d * 128 : d * 256) + cc * 16;
        u32x4 v = *(const u32x4*)g;
        *(u32x4*)(lds + m * 16384 + c * 16) = v;
      }
    }
  }
  int batch = gw;
  bf16x8 En[8];
#pragma unroll
  for (int s = 0; s < 8; ++s) {
    u32x4 v = __builtin_amdgcn_raw_buffer_load_b128(ers, lane_in + (unsigned)batch * BATCHB + s * 32, 0, 0);
    En[s] = __builtin_bit_cast(bf16x8, v);
  }
  __syncthreads();
  bf16x8 W[4][2][8];
  {
    const int g16 = lane >> 4, i16 = lane & 15, q = i16 >> 2, p = i16 & 3;
    typedef __attribute__((address_space(3))) v4i16_t* lptr;
    const unsigned base = (unsigned)(size_t)lds + (8 * (g16 >> 1) + q) * 128 + (16 * (g16 & 1) + 4 * p) * 2;
#pragma unroll
    for (int m = 0; m < 4; ++m)
#pragma unroll
      for (int t = 0; t < 2; ++t)
#pragma unroll
        for (int s = 0; s < 8; ++s) {
          const unsigned a = base + m * 16384 + s * 16 * 128 + t * 64;
          v4i16_t lo = __builtin_amdgcn_ds_read_tr16_b64_v4i16((lptr)(size_t)a);
          v4i16_t hi = __builtin_amdgcn_ds_read_tr16_b64_v4i16((lptr)(size_t)(a + 4 * 128));
          typedef short s16x8 __attribute__((ext_vector_type(8)));
          s16x8 w8 = __builtin_shufflevector(lo, hi, 0, 1, 2, 3, 4, 5, 6, 7);
          W[m][t][s] = __builtin_bit_cast(bf16x8, w8);
        }
  }

  const float C2 = 0.125f * 1.4426950408889634f;
  const f32x16 zero = {};
  for (; batch < B; batch += nw) {
    bf16x8 E[8];
#pragma unroll
    for (int s = 0; s < 8; ++s) E[s] = En[s];
    {
      const unsigned nb = (unsigned)(batch + nw);
#pragma unroll
      for (int s = 0; s < 8; ++s) {
        u32x4 v = __builtin_amdgcn_raw_buffer_load_b128(ers, lane_in + nb * BATCHB + s * 32, 0, 0);
        En[s] = __builtin_bit_cast(bf16x8, v);
      }
    }
    f32x16 aq0 = zero, aq1 = zero, ak0 = zero, ak1 = zero;
#pragma unroll
    for (int s = 0; s < 8; ++s) aq0 = MFMA(W[0][0][s], E[s], aq0);
#pragma unroll
    for (int s = 0; s < 8; ++s) aq1 = MFMA(W[0][1][s], E[s], aq1);
#pragma unroll
    for (int s = 0; s < 8; ++s) ak0 = MFMA(W[1][0][s], E[s], ak0);
#pragma unroll
    for (int s = 0; s < 8; ++s) ak1 = MFMA(W[1][1][s], E[s], ak1);
    f32x16 av0 = zero, av1 = zero;
#pragma unroll
    for (int s = 0; s < 8; ++s) av0 = MFMA(E[s], W[2][0][s], av0);
    f32x16 sc = zero;
    sc = MFMA(pack8(ak0, 0), pack8(aq0, 0), sc);
    sc = MFMA(pack8(ak0, 1), pack8(aq0, 1), sc);
    sc = MFMA(pack8(ak1, 0), pack8(aq1, 0), sc);
    sc = MFMA(pack8(ak1, 1), pack8(aq1, 1), sc);
#pragma unroll
    for (int s = 0; s < 8; ++s) av1 = MFMA(E[s], W[2][1][s], av1);
    f32x16 ao0 = zero, ao1 = zero;
#pragma unroll
    for (int s = 0; s < 8; ++s) ao0 = MFMA(W[3][0][s], E[s], ao0);
#pragma unroll
    for (int s = 0; s < 8; ++s) ao1 = MFMA(W[3][1][s], E[s], ao1);
    float x[14];
#pragma unroll
    for (int i = 0; i < 14; i += 2) {
      unsigned p = pk2(sc[i], sc[i + 1]);
      x[i] = __uint_as_float(p << 16);
      x[i + 1] = __uint_as_float(p & 0xffff0000u);
    }
    if (h) { x[12] = -INFINITY; x[13] = -INFINITY; }
    float m = __builtin_fmaxf(x[0], x[1]);
#pragma unroll
    for (int i = 2; i < 14; ++i) m = __builtin_fmaxf(m, x[i]);
    m = swapmax(m);
    const float nm = -m * C2;
    float sum = 0.f;
#pragma unroll
    for (int i = 0; i < 14; ++i) {
      x[i] = __builtin_amdgcn_exp2f(__builtin_fmaf(x[i], C2, nm));
      sum += x[i];
    }
    sum = swapadd(sum);
    const float inv = __builtin_amdgcn_rcpf(sum);
    f32x16 pr;
#pragma unroll
    for (int i = 0; i < 14; ++i) pr[i] = x[i] * inv;
    pr[14] = 0.f; pr[15] = 0.f;
    const bf16x8 p0 = pack8(pr, 0), p1 = pack8(pr, 1);
    ao0 = MFMA(pack8(av0, 0), p0, ao0);
    ao0 = MFMA(pack8(av0, 1), p1, ao0);
    ao1 = MFMA(pack8(av1, 0), p0, ao1);
    ao1 = MFMA(pack8(av1, 1), p1, ao1);
    const unsigned ob = lane_out + (unsigned)batch * BATCHB;
#pragma unroll
    for (int t = 0; t < 2; ++t) {
      const f32x16& a = t ? ao1 : ao0;
#pragma unroll
      for (int mm = 0; mm < 2; ++mm) {
        unsigned ax = pk2(__builtin_fmaxf(a[8 * mm + 0], 0.f), __builtin_fmaxf(a[8 * mm + 1], 0.f));
        unsigned ay = pk2(__builtin_fmaxf(a[8 * mm + 2], 0.f), __builtin_fmaxf(a[8 * mm + 3], 0.f));
        unsigned bx = pk2(__builtin_fmaxf(a[8 * mm + 4], 0.f), __builtin_fmaxf(a[8 * mm + 5], 0.f));
        unsigned by = pk2(__builtin_fmaxf(a[8 * mm + 6], 0.f), __builtin_fmaxf(a[8 * mm + 7], 0.f));
        auto rx = __builtin_amdgcn_permlane32_swap(ax, bx, false, false);
        auto ry = __builtin_amdgcn_permlane32_swap(ay, by, false, false);
        u32x4 v = {rx[0], ry[0], rx[1], ry[1]};
        __builtin_amdgcn_raw_buffer_store_b128(v, ors, ob + t * 64 + mm * 32, 0, 0);
      }
    }
  }
}

extern "C" void kernel_launch(void* const* d_in, const int* in_sizes, int n_in, void* d_out, int out_size,
                              void* d_ws, size_t ws_size, hipStream_t stream) {
  const __hip_bfloat16* e = (const __hip_bfloat16*)d_in[0];
  const __hip_bfloat16* wq = (const __hip_bfloat16*)d_in[1];
  const __hip_bfloat16* wk = (const __hip_bfloat16*)d_in[2];
  const __hip_bfloat16* wv = (const __hip_bfloat16*)d_in[3];
  const __hip_bfloat16* wres = (const __hip_bfloat16*)d_in[4];
  __hip_bfloat16* out = (__hip_bfloat16*)d_out;
  const int B = in_sizes[0] / (F_ * D_);
  autoint_kernel<<<256, 256, 0, stream>>>(e, wq, wk, wv, wres, out, B);
}
```

```cpp
#include <hip/hip_runtime.h>
#include <hip/hip_bf16.h>

#define STPOL " sc1"
constexpr int F_ = 26, D_ = 128;

__global__ __launch_bounds__(256, 1) __attribute__((aligned(4096))) void autoint_kernel(const __hip_bfloat16* __restrict__ e,
                                                          const __hip_bfloat16* __restrict__ wq,
                                                          const __hip_bfloat16* __restrict__ wk,
                                                          const __hip_bfloat16* __restrict__ wv,
                                                          const __hip_bfloat16* __restrict__ wres,
                                                          __hip_bfloat16* __restrict__ out, int B
) {
  __shared__ __attribute__((aligned(1024))) char lds[163840];
  const unsigned lb = (unsigned)(size_t)lds + 9984, bid = blockIdx.x, tid = threadIdx.x;
  const auto ka = __builtin_amdgcn_kernarg_segment_ptr();
  asm volatile(
    "s_load_dwordx2 s[4:5], %0, 0x8\n\t"
    "s_load_dwordx2 s[20:21], %0, 0x0\n\t"
    "s_load_dwordx2 s[8:9], %0, 0x10\n\t"
    "s_load_dwordx2 s[12:13], %0, 0x18\n\t"
    "s_load_dwordx2 s[16:17], %0, 0x20\n\t"
    "s_load_dwordx2 s[24:25], %0, 0x28\n\t"
    "s_load_dword s28, %0, 0x30\n\t"
    "s_getpc_b64 s[66:67]\n\t"
    "s_lshr_b32 s64, %1, 3\n\t"
    "v_lshlrev_b32 v22, 7, %2\n\t"
    "v_xor_b32 v23, s64, %2\n\t"
    "v_and_b32 v23, 15, v23\n\t"
    "v_cmp_eq_u32 s[68:69], 0, v23\n\t"
    "v_cmp_gt_u32 vcc, 92, %2\n\t"
    "s_or_b64 s[68:69], s[68:69], vcc\n\t"
    "v_cmp_gt_u32 vcc, 174, %2\n\t"
    "s_and_b64 vcc, vcc, s[68:69]\n\t"
    "v_cndmask_b32 v22, 0, v22, vcc\n\t"
    "global_load_dword v31, v22, s[66:67]\n\t"
    "v_readfirstlane_b32 s29, %2\n\t"
    "s_and_b32 s64, s64, 1\n\t"
    "s_lshr_b32 s29, s29, 6\n\t"
    "s_lshl_b32 s30, s29, 12\n\t"
    "s_add_u32 s31, s30, %3\n\t"
    "s_lshl_b32 s65, s64, 14\n\t"
    "s_add_u32 s30, s30, s65\n\t"
    "s_mov_b32 s6, 32768\n\t"
    "s_mov_b32 s7, 131072\n\t"
    "v_bfe_u32 v22, %2, 3, 3\n\t"
    "v_and_b32 v23, 7, %2\n\t"
    "v_and_b32 v24, 2, v22\n\t"
    "v_xor_b32 v24, v24, v23\n\t"
    "v_lshlrev_b32 v24, 4, v24\n\t"
    "v_lshl_or_b32 v1, v22, 7, v24\n\t"
    "v_xor_b32 v2, 64, v1\n\t"
    "v_and_b32 v25, 15, %2\n\t"
    "v_bfe_u32 v26, %2, 4, 2\n\t"
    "v_xor_b32 v24, v25, v26\n\t"
    "v_lshlrev_b32 v24, 4, v24\n\t"
    "v_lshl_or_b32 v251, v26, 8, v24\n\t"
    "v_add_u32 v248, 192, v251\n\t"
    "v_add_u32 v249, 128, v251\n\t"
    "v_add_u32 v250, 64, v251\n\t"
    "s_mul_i32 s33, s29, 21760\n\t"
    "s_add_u32 s33, s33, %3\n\t"
    "s_add_u32 s33, s33, 65792\n\t"
    "s_lshl_b32 s73, %1, 4\n\t"
    "s_and_b32 s65, s73, 4294963200\n\t"
    "s_and_b32 s73, s73, 112\n\t"
    "s_lshr_b32 s72, %1, 4\n\t"
    "s_and_b32 s72, s72, 15\n\t"
    "s_or_b32 s73, s73, s72\n\t"
    "s_lshl_b32 s73, s73, 2\n\t"
    "s_or_b32 s73, s73, s65\n\t"
    "s_add_u32 s73, s73, s29\n\t"
    "s_mul_i32 s35, s73, 6656\n\t"
    "s_mul_hi_u32 s36, s73, 6656\n\t"
    "s_sub_u32 s70, s35, 192\n\t"
    "s_subb_u32 s71, s36, 0\n\t"
    "s_mov_b32 s23, 131072\n\t"
    "s_mov_b32 s37, 0\n\t"
    "s_lshr_b32 s72, %1, 1\n\t"
    "s_and_b32 s72, s72, 120\n\t"
    "v_or_b32 v192, s72, v23\n\t"
    "s_cmp_eq_u32 s29, 3\n\t"
    "s_cselect_b32 s72, 8, 7\n\t"
    "s_cselect_b32 s65, 7, 14\n\t"
    "s_lshl_b32 s65, s64, s65\n\t"
    "v_lshlrev_b32 v192, s72, v192\n\t"
    "v_add_u32 v192, s65, v192\n\t"
    "v_mov_b32 v27, 3401216\n\t"
    "v_and_b32 v24, 32, %2\n\t"
    "v_cmp_ne_u32 vcc, 0, v24\n\t"
    "v_cndmask_b32 v24, 0, v27, vcc\n\t"
    "v_add_u32 v18, v248, v24\n\t"
    "v_add_u32 v19, v249, v24\n\t"
    "v_add_u32 v20, v250, v24\n\t"
    "v_add_u32 v21, v251, v24\n\t"
    "s_mov_b32 s38, 4096\n\t"
    "s_mov_b32 s39, 3405312\n\t"
    "s_mov_b32 s40, 3409408\n\t"
    "s_mov_b32 s41, 3413504\n\t"
    "s_mov_b32 s42, 6814720\n\t"
    "s_mov_b32 s43, 6818816\n\t"
    "s_mov_b32 s44, 10220032\n\t"
    "s_mov_b32 s10, 32768\n\t"
    "s_mov_b32 s11, 131072\n\t"
    "s_mov_b32 s14, 32768\n\t"
    "s_mov_b32 s15, 131072\n\t"
    "s_mov_b32 s18, 32768\n\t"
    "s_mov_b32 s19, 131072\n\t"
    "v_bfe_u32 v28, %2, 2, 2\n\t"
    "v_and_b32 v29, 3, %2\n\t"
    "v_lshlrev_b32 v24, 10, v26\n\t"
    "v_lshl_or_b32 v24, v28, 7, v24\n\t"
    "v_lshl_or_b32 v24, v29, 3, v24\n\t"
    "v_add_u32 v24, %3, v24\n\t"
    "v_lshlrev_b32 v30, 2, %2\n\t"
    "v_and_b32 v30, 96, v30\n\t"
    "v_add_u32 v5, v24, v30\n\t"
    "v_xor_b32 v6, 32, v30\n\t"
    "v_add_u32 v6, v6, v24\n\t"
    "v_xor_b32 v7, 64, v30\n\t"
    "v_add_u32 v7, v7, v24\n\t"
    "v_xor_b32 v8, 96, v30\n\t"
    "v_add_u32 v8, v8, v24\n\t"
    "v_xor_b32 v24, v26, v29\n\t"
    "v_lshlrev_b32 v24, 4, v24\n\t"
    "v_lshl_or_b32 v24, v29, 8, v24\n\t"
    "v_mul_u32_u24 v30, 1088, v28\n\t"
    "v_add_u32 v24, v24, v30\n\t"
    "s_add_u32 m0, s31, 0\n\t"
    "s_waitcnt lgkmcnt(0)\n\t"
    "buffer_load_dwordx4 v1, s[4:7], s30 offen offset:0 lds\n\t"
    "s_add_u32 s20, s20, s70\n\t"
    "s_addc_u32 s21, s21, s71\n\t"
    "buffer_load_dwordx4 v2, s[4:7], s30 offen offset:1024 lds\n\t"
    "s_sub_i32 s28, s28, s73\n\t"
    "s_max_i32 s28, s28, 0\n\t"
    "s_min_i32 s28, s28, 4096\n\t"
    "buffer_load_dwordx4 v1, s[4:7], s30 offen offset:2048 lds\n\t"
    "s_mul_i32 s26, s28, 6656\n\t"
    "s_add_u32 s22, s26, 192\n\t"
    "buffer_load_dwordx4 v2, s[4:7], s30 offen offset:3072 lds\n\t"
    "s_add_u32 m0, s33, 0\n\t"
    "s_nop 0\n\t"
    "buffer_load_dwordx4 v248, s[20:23], s37 offen offset:0 nt lds\n\t"
    "buffer_load_dwordx4 v249, s[20:23], s37 offen offset:1088 nt lds\n\t"
    "buffer_load_dwordx4 v250, s[20:23], s37 offen offset:2176 nt lds\n\t"
    "buffer_load_dwordx4 v251, s[20:23], s37 offen offset:3264 nt lds\n\t"
    "v_add_u32 v11, s33, v24\n\t"
    "s_mul_i32 s34, s29, 4352\n\t"
    "s_add_u32 s34, s34, %3\n\t"
    "s_add_u32 s34, s34, 4294957312\n\t"
    "v_add_u32 v14, s34, v24\n\t"
    "v_and_b32 v24, 1, v22\n\t"
    "v_lshl_or_b32 v24, v26, 2, v24\n\t"
    "v_and_b32 v30, 4, v22\n\t"
    "v_xor_b32 v30, v30, v23\n\t"
    "v_lshlrev_b32 v30, 4, v30\n\t"
    "v_lshl_or_b32 v3, v24, 7, v30\n\t"
    "v_lshl_or_b32 v4, v24, 8, v30\n\t"
    "v_add_u32 v64, 4096, v4\n\t"
    "s_lshl_b32 s65, s29, 13\n\t"
    "s_lshl_b32 s72, s64, 7\n\t"
    "s_add_u32 s65, s65, s72\n\t"
    "s_mul_i32 s64, s29, 4128\n\t"
    "s_add_u32 s64, s64, %3\n\t"
    "s_mov_b32 s66, 2167144808\n\t"
    "s_mov_b32 s67, 52\n\t"
    "s_mov_b32 s68, 2150924588\n\t"
    "s_mov_b32 s69, 22\n\t"
    "v_mov_b32 v72, 0xff800000\n\t"
    "v_cndmask_b32 v68, 0, v72, s[66:67]\n\t"
    "v_cndmask_b32 v69, 0, v72, s[66:67]\n\t"
    "v_cndmask_b32 v70, 0, v72, s[68:69]\n\t"
    "v_cndmask_b32 v71, 0, v72, s[68:69]\n\t"
    "s_add_u32 s66, %3, 152832\n\t"
    "v_and_b32 v24, 63, %2\n\t"
    "v_lshl_add_u32 v24, v24, 4, s66\n\t"
    "ds_write_b128 v24, v[68:71]\n\t"
    "v_lshl_add_u32 v13, v26, 4, s66\n\t"
    "v_and_b32 v24, 2, v26\n\t"
    "v_lshrrev_b32 v30, 1, v28\n\t"
    "v_add_u32 v24, v24, v30\n\t"
    "v_mul_u32_u24 v24, 1032, v24\n\t"
    "v_lshl_add_u32 v24, v29, 4, v24\n\t"
    "v_add_u32 v24, %3, v24\n\t"
    "v_and_b32 v30, 1, v28\n\t"
    "v_lshl_add_u32 v24, v30, 7, v24\n\t"
    "v_and_b32 v30, 1, v26\n\t"
    "v_lshl_add_u32 v24, v30, 9, v24\n\t"
    "v_lshlrev_b32 v30, 6, v30\n\t"
    "v_add_u32 v9, v24, v30\n\t"
    "v_xor_b32 v30, 64, v30\n\t"
    "v_add_u32 v10, v24, v30\n\t"
    "v_and_b32 v24, 8, %2\n\t"
    "v_lshlrev_b32 v24, 3, v24\n\t"
    "v_lshl_or_b32 v24, v23, 8, v24\n\t"
    "v_lshl_or_b32 v24, v26, 4, v24\n\t"
    "v_add_u32 v12, s72, v24\n\t"
    "v_add_u32 v24, v12, v27\n\t"
    "v_cmp_lt_u32 vcc, 1, v23\n\t"
    "v_cndmask_b32 v15, v12, v24, vcc\n\t"
    "v_cmp_lt_u32 vcc, 3, v23\n\t"
    "v_cndmask_b32 v16, v12, v24, vcc\n\t"
    "v_cmp_lt_u32 vcc, 5, v23\n\t"
    "v_cndmask_b32 v17, v12, v24, vcc\n\t"
    "s_mov_b32 s27, 131072\n\t"
    "s_mov_b32 s50, 196611\n\t"
    "s_mov_b32 s51, 196611\n\t"
    "s_mov_b32 s52, 983055\n\t"
    "s_mov_b32 s53, 983055\n\t"
    "s_mov_b32 s54, 4128831\n\t"
    "s_mov_b32 s55, 4128831\n\t"
    "s_mov_b32 s56, 16711935\n\t"
    "s_mov_b32 s57, 16711935\n\t"
    "s_mov_b32 s58, 67044351\n\t"
    "s_mov_b32 s59, 67044351\n\t"
    "s_mov_b32 s60, 268374015\n\t"
    "s_mov_b32 s61, 268374015\n\t"
    "s_mov_b32 s62, 1073692671\n\t"
    "s_mov_b32 s63, 1073692671\n\t"
    "s_add_u32 s24, s24, s35\n\t"
    "s_addc_u32 s25, s25, s36\n\t"
    "s_mov_b32 s45, 10224128\n\t"
    "s_mov_b32 s46, 10228224\n\t"
    "s_mov_b32 s47, 13629440\n\t"
    "s_mov_b32 s48, 13633536\n\t"
    "s_mov_b32 s49, 13637632\n\t"
    "s_mov_b32 s66, 17038848\n\t"
    "s_mov_b32 s67, 17042944\n\t"
    "s_mov_b32 s68, 20444160\n\t"
    "s_mov_b32 s69, 20448256\n\t"
    "s_mov_b32 s70, 20452352\n\t"
    "s_mov_b32 s71, 23853568\n\t"
    "s_mov_b32 s72, 23857664\n\t"
    "s_add_u32 m0, s31, 16384\n\t"
    "s_nop 0\n\t"
    "buffer_load_dwordx4 v1, s[8:11], s30 offen offset:0 lds\n\t"
    "buffer_load_dwordx4 v2, s[8:11], s30 offen offset:1024 lds\n\t"
    "buffer_load_dwordx4 v1, s[8:11], s30 offen offset:2048 lds\n\t"
    "buffer_load_dwordx4 v2, s[8:11], s30 offen offset:3072 lds\n\t"
    "s_cmp_eq_u32 s29, 1\n\t"
    "s_cselect_b64 vcc, s[8:9], s[12:13]\n\t"
    "s_cmp_eq_u32 s29, 3\n\t"
    "s_cselect_b64 vcc, s[16:17], vcc\n\t"
    "global_load_dword v31, v192, vcc\n\t"
    "s_mov_b32 vcc_lo, 0xff00ff\n\t"
    "s_mov_b32 vcc_hi, 0xff00ff\n\t"
    "s_mov_b64 exec, 0\n\t"
    "s_cmp_eq_u32 s29, 0\n\t"
    "s_cbranch_scc1 .Ls0_%=\n\t"
    "s_cmp_eq_u32 s29, 1\n\t"
    "s_cbranch_scc1 .Ls1_%=\n\t"
    "s_cmp_eq_u32 s29, 2\n\t"
    "s_cbranch_scc1 .Ls2_%=\n\t"
    "s_cmp_eq_u32 s29, 3\n\t"
    "s_cbranch_scc1 .Ls3_%=\n\t"
    ".Lret%=:\n\t"
    "s_mov_b64 exec, -1\n\t"
    "s_add_u32 m0, s33, 4352\n\t"
    "s_nop 0\n\t"
    "buffer_load_dwordx4 v248, s[20:23], s38 offen offset:0 nt lds\n\t"
    "buffer_load_dwordx4 v249, s[20:23], s38 offen offset:1088 nt lds\n\t"
    "buffer_load_dwordx4 v20, s[20:23], s38 offen offset:2176 nt lds\n\t"
    "buffer_load_dwordx4 v251, s[20:23], s39 offen offset:3264 nt lds\n\t"
    "s_waitcnt vmcnt(13)\n\t"
    "s_barrier\n\t"
    "ds_read_b64_tr_b16 a[0:1], v5 offset:0\n\t"
    "ds_read_b64_tr_b16 a[2:3], v5 offset:512\n\t"
    "ds_read_b64_tr_b16 a[4:5], v5 offset:4096\n\t"
    "ds_read_b64_tr_b16 a[6:7], v5 offset:4608\n\t"
    "ds_read_b64_tr_b16 a[8:9], v5 offset:8192\n\t"
    "ds_read_b64_tr_b16 a[10:11], v5 offset:8704\n\t"
    "ds_read_b64_tr_b16 a[12:13], v5 offset:12288\n\t"
    "ds_read_b64_tr_b16 a[14:15], v5 offset:12800\n\t"
    "ds_read_b64_tr_b16 a[28:29], v6 offset:12288\n\t"
    "ds_read_b64_tr_b16 a[30:31], v6 offset:12800\n\t"
    "ds_read_b64_tr_b16 a[24:25], v6 offset:8192\n\t"
    "ds_read_b64_tr_b16 a[26:27], v6 offset:8704\n\t"
    "ds_read_b64_tr_b16 a[20:21], v6 offset:4096\n\t"
    "ds_read_b64_tr_b16 a[22:23], v6 offset:4608\n\t"
    "ds_read_b64_tr_b16 a[16:17], v6 offset:0\n\t"
    "ds_read_b64_tr_b16 a[18:19], v6 offset:512\n\t"
    "ds_read_b64_tr_b16 a[32:33], v7 offset:0\n\t"
    "ds_read_b64_tr_b16 a[34:35], v7 offset:512\n\t"
    "ds_read_b64_tr_b16 a[36:37], v7 offset:4096\n\t"
    "ds_read_b64_tr_b16 a[38:39], v7 offset:4608\n\t"
    "ds_read_b64_tr_b16 a[40:41], v7 offset:8192\n\t"
    "ds_read_b64_tr_b16 a[42:43], v7 offset:8704\n\t"
    "ds_read_b64_tr_b16 a[44:45], v7 offset:12288\n\t"
    "ds_read_b64_tr_b16 a[46:47], v7 offset:12800\n\t"
    "ds_read_b64_tr_b16 a[60:61], v8 offset:12288\n\t"
    "ds_read_b64_tr_b16 a[62:63], v8 offset:12800\n\t"
    "ds_read_b64_tr_b16 a[56:57], v8 offset:8192\n\t"
    "ds_read_b64_tr_b16 a[58:59], v8 offset:8704\n\t"
    "ds_read_b64_tr_b16 a[52:53], v8 offset:4096\n\t"
    "ds_read_b64_tr_b16 a[54:55], v8 offset:4608\n\t"
    "ds_read_b64_tr_b16 a[48:49], v8 offset:0\n\t"
    "ds_read_b64_tr_b16 a[50:51], v8 offset:512\n\t"
    "s_waitcnt vmcnt(9)\n\t"
    "ds_read_b128 v[32:35], v11 offset:0\n\t"
    "ds_read_b128 v[36:39], v11 offset:64\n\t"
    "ds_read_b128 v[40:43], v11 offset:128\n\t"
    "ds_read_b128 v[44:47], v11 offset:192\n\t"
    "ds_read_b128 v[212:215], v13 offset:0\n\t"
    "s_waitcnt lgkmcnt(4)\n\t"
    "v_mfma_f32_16x16x32_bf16 v[216:219], a[0:3], v[32:35], 0\n\t"
    "s_add_u32 m0, s64, 32768\n\t"
    "s_waitcnt lgkmcnt(3)\n\t"
    "v_mfma_f32_16x16x32_bf16 v[216:219], a[4:7], v[36:39], v[216:219]\n\t"
    "buffer_load_dwordx4 v3, s[12:15], s30 offen offset:0 lds\n\t"
    "s_waitcnt lgkmcnt(2)\n\t"
    "v_mfma_f32_16x16x32_bf16 v[216:219], a[8:11], v[40:43], v[216:219]\n\t"
    "s_add_u32 m0, s64, 33544\n\t"
    "s_waitcnt lgkmcnt(1)\n\t"
    "v_mfma_f32_16x16x32_bf16 v[216:219], a[12:15], v[44:47], v[216:219]\n\t"
    "v_mfma_f32_16x16x32_bf16 v[220:223], a[28:31], v[44:47], 0\n\t"
    "buffer_load_dwordx4 v3, s[12:15], s30 offen offset:256 lds\n\t"
    "v_mfma_f32_16x16x32_bf16 v[220:223], a[24:27], v[40:43], v[220:223]\n\t"
    "s_add_u32 m0, s64, 32784\n\t"
    "v_mfma_f32_16x16x32_bf16 v[220:223], a[20:23], v[36:39], v[220:223]\n\t"
    "v_mfma_f32_16x16x32_bf16 v[220:223], a[16:19], v[32:35], v[220:223]\n\t"
    "buffer_load_dwordx4 v3, s[12:15], s30 offen offset:2048 lds\n\t"
    "v_cvt_pk_bf16_f32 v112, v216, v217\n\t"
    "v_mfma_f32_16x16x32_bf16 v[224:227], a[32:35], v[32:35], 0\n\t"
    "s_waitcnt vmcnt(8)\n\t"
    "s_barrier\n\t"
    "ds_read_b64_tr_b16 a[64:65], v5 offset:16384\n\t"
    "ds_read_b64_tr_b16 a[66:67], v5 offset:16896\n\t"
    "s_add_u32 m0, s64, 33560\n\t"
    "v_mfma_f32_16x16x32_bf16 v[224:227], a[36:39], v[36:39], v[224:227]\n\t"
    "ds_read_b64_tr_b16 a[68:69], v5 offset:20480\n\t"
    "ds_read_b64_tr_b16 a[70:71], v5 offset:20992\n\t"
    "v_mfma_f32_16x16x32_bf16 v[224:227], a[40:43], v[40:43], v[224:227]\n\t"
    "ds_read_b64_tr_b16 a[72:73], v5 offset:24576\n\t"
    "ds_read_b64_tr_b16 a[74:75], v5 offset:25088\n\t"
    "buffer_load_dwordx4 v3, s[12:15], s30 offen offset:2304 lds\n\t"
    "v_mfma_f32_16x16x32_bf16 v[224:227], a[44:47], v[44:47], v[224:227]\n\t"
    "ds_read_b64_tr_b16 a[76:77], v5 offset:28672\n\t"
    "ds_read_b64_tr_b16 a[78:79], v5 offset:29184\n\t"
    "s_add_u32 m0, s64, 49280\n\t"
    "v_mfma_f32_16x16x32_bf16 v[228:231], a[60:63], v[44:47], 0\n\t"
    "ds_read_b64_tr_b16 a[92:93], v6 offset:28672\n\t"
    "ds_read_b64_tr_b16 a[94:95], v6 offset:29184\n\t"
    "v_mfma_f32_16x16x32_bf16 v[228:231], a[56:59], v[40:43], v[228:231]\n\t"
    "ds_read_b64_tr_b16 a[88:89], v6 offset:24576\n\t"
    "ds_read_b64_tr_b16 a[90:91], v6 offset:25088\n\t"
    "buffer_load_dwordx4 v4, s[16:19], s65 offen offset:0 lds\n\t"
    "v_mfma_f32_16x16x32_bf16 v[228:231], a[52:55], v[36:39], v[228:231]\n\t"
    "ds_read_b64_tr_b16 a[84:85], v6 offset:20480\n\t"
    "ds_read_b64_tr_b16 a[86:87], v6 offset:20992\n\t"
    "s_add_u32 m0, s64, 49800\n\t"
    "v_mfma_f32_16x16x32_bf16 v[228:231], a[48:51], v[32:35], v[228:231]\n\t"
    "ds_read_b64_tr_b16 a[80:81], v6 offset:16384\n\t"
    "ds_read_b64_tr_b16 a[82:83], v6 offset:16896\n\t"
    "s_waitcnt lgkmcnt(14)\n\t"
    "v_mfma_f32_16x16x32_bf16 v[232:235], a[64:67], v[32:35], 0\n\t"
    "ds_read_b64_tr_b16 a[96:97], v7 offset:16384\n\t"
    "ds_read_b64_tr_b16 a[98:99], v7 offset:16896\n\t"
    "buffer_load_dwordx4 v4, s[16:19], s65 offen offset:512 lds\n\t"
    "s_waitcnt lgkmcnt(14)\n\t"
    "v_mfma_f32_16x16x32_bf16 v[232:235], a[68:71], v[36:39], v[232:235]\n\t"
    "ds_read_b64_tr_b16 a[100:101], v7 offset:20480\n\t"
    "ds_read_b64_tr_b16 a[102:103], v7 offset:20992\n\t"
    "s_add_u32 m0, s64, 51344\n\t"
    "s_waitcnt lgkmcnt(14)\n\t"
    "v_mfma_f32_16x16x32_bf16 v[232:235], a[72:75], v[40:43], v[232:235]\n\t"
    "ds_read_b64_tr_b16 a[104:105], v7 offset:24576\n\t"
    "ds_read_b64_tr_b16 a[106:107], v7 offset:25088\n\t"
    "s_waitcnt lgkmcnt(14)\n\t"
    "v_mfma_f32_16x16x32_bf16 v[232:235], a[76:79], v[44:47], v[232:235]\n\t"
    "ds_read_b64_tr_b16 a[108:109], v7 offset:28672\n\t"
    "ds_read_b64_tr_b16 a[110:111], v7 offset:29184\n\t"
    "buffer_load_dwordx4 v64, s[16:19], s65 offen offset:0 lds\n\t"
    "s_waitcnt lgkmcnt(14)\n\t"
    "v_mfma_f32_16x16x32_bf16 v[236:239], a[92:95], v[44:47], 0\n\t"
    "ds_read_b64_tr_b16 a[124:125], v8 offset:28672\n\t"
    "ds_read_b64_tr_b16 a[126:127], v8 offset:29184\n\t"
    "s_add_u32 m0, s64, 51864\n\t"
    "s_waitcnt lgkmcnt(14)\n\t"
    "v_mfma_f32_16x16x32_bf16 v[236:239], a[88:91], v[40:43], v[236:239]\n\t"
    "ds_read_b64_tr_b16 a[120:121], v8 offset:24576\n\t"
    "ds_read_b64_tr_b16 a[122:123], v8 offset:25088\n\t"
    "s_waitcnt lgkmcnt(14)\n\t"
    "v_mfma_f32_16x16x32_bf16 v[236:239], a[84:87], v[36:39], v[236:239]\n\t"
    "ds_read_b64_tr_b16 a[116:117], v8 offset:20480\n\t"
    "ds_read_b64_tr_b16 a[118:119], v8 offset:20992\n\t"
    "buffer_load_dwordx4 v64, s[16:19], s65 offen offset:512 lds\n\t"
    "s_waitcnt lgkmcnt(14)\n\t"
    "v_mfma_f32_16x16x32_bf16 v[236:239], a[80:83], v[32:35], v[236:239]\n\t"
    "ds_read_b64_tr_b16 a[112:113], v8 offset:16384\n\t"
    "ds_read_b64_tr_b16 a[114:115], v8 offset:16896\n\t"
    "s_add_u32 m0, s33, 8704\n\t"
    "v_cvt_pk_bf16_f32 v113, v218, v219\n\t"
    "s_waitcnt lgkmcnt(14)\n\t"
    "v_mfma_f32_16x16x32_bf16 v[216:219], a[96:99], v[32:35], 0\n\t"
    "v_cvt_pk_bf16_f32 v114, v220, v221\n\t"
    "v_cvt_pk_bf16_f32 v115, v222, v223\n\t"
    "s_waitcnt lgkmcnt(12)\n\t"
    "v_mfma_f32_16x16x32_bf16 v[216:219], a[100:103], v[36:39], v[216:219]\n\t"
    "v_cvt_pk_bf16_f32 v116, v224, v225\n\t"
    "v_cvt_pk_bf16_f32 v117, v226, v227\n\t"
    "s_waitcnt lgkmcnt(10)\n\t"
    "v_mfma_f32_16x16x32_bf16 v[216:219], a[104:107], v[40:43], v[216:219]\n\t"
    "s_waitcnt vmcnt(8)\n\t"
    "ds_read_b128 v[48:51], v11 offset:4352\n\t"
    "ds_read_b128 v[52:55], v11 offset:4416\n\t"
    "ds_read_b128 v[56:59], v11 offset:4480\n\t"
    "ds_read_b128 v[60:63], v11 offset:4544\n\t"
    "s_waitcnt lgkmcnt(12)\n\t"
    "v_mfma_f32_16x16x32_bf16 v[216:219], a[108:111], v[44:47], v[216:219]\n\t"
    "v_cvt_pk_bf16_f32 v118, v228, v229\n\t"
    "v_cvt_pk_bf16_f32 v119, v230, v231\n\t"
    "s_waitcnt lgkmcnt(10)\n\t"
    "v_mfma_f32_16x16x32_bf16 v[220:223], a[124:127], v[44:47], 0\n\t"
    "v_cvt_pk_bf16_f32 v136, v232, v233\n\t"
    "v_cvt_pk_bf16_f32 v137, v234, v235\n\t"
    "s_waitcnt lgkmcnt(8)\n\t"
    "v_mfma_f32_16x16x32_bf16 v[220:223], a[120:123], v[40:43], v[220:223]\n\t"
    "buffer_load_dwordx4 v248, s[20:23], s40 offen offset:0 nt lds\n\t"
    "v_cvt_pk_bf16_f32 v138, v236, v237\n\t"
    "s_waitcnt lgkmcnt(6)\n\t"
    "v_mfma_f32_16x16x32_bf16 v[220:223], a[116:119], v[36:39], v[220:223]\n\t"
    "v_cvt_pk_bf16_f32 v139, v238, v239\n\t"
    "s_waitcnt lgkmcnt(4)\n\t"
    "v_mfma_f32_16x16x32_bf16 v[220:223], a[112:115], v[32:35], v[220:223]\n\t"
    "v_cvt_pk_bf16_f32 v140, v216, v217\n\t"
    "v_cvt_pk_bf16_f32 v141, v218, v219\n\t"
    "s_waitcnt lgkmcnt(3)\n\t"
    "v_mfma_f32_16x16x32_bf16 v[224:227], a[0:3], v[48:51], 0\n\t"
    "s_waitcnt lgkmcnt(2)\n\t"
    "v_mfma_f32_16x16x32_bf16 v[224:227], a[4:7], v[52:55], v[224:227]\n\t"
    "s_waitcnt lgkmcnt(1)\n\t"
    "v_mfma_f32_16x16x32_bf16 v[224:227], a[8:11], v[56:59], v[224:227]\n\t"
    "s_waitcnt lgkmcnt(0)\n\t"
    "v_mfma_f32_16x16x32_bf16 v[224:227], a[12:15], v[60:63], v[224:227]\n\t"
    "v_cvt_pk_bf16_f32 v142, v220, v221\n\t"
    "v_cvt_pk_bf16_f32 v143, v222, v223\n\t"
    "v_mfma_f32_16x16x32_bf16 v[228:231], a[28:31], v[60:63], 0\n\t"
    "buffer_load_dwordx4 v249, s[20:23], s40 offen offset:1088 nt lds\n\t"
    "v_mfma_f32_16x16x32_bf16 v[228:231], a[24:27], v[56:59], v[228:231]\n\t"
    "v_mfma_f32_16x16x32_bf16 v[228:231], a[20:23], v[52:55], v[228:231]\n\t"
    "v_mfma_f32_16x16x32_bf16 v[228:231], a[16:19], v[48:51], v[228:231]\n\t"
    "v_cvt_pk_bf16_f32 v120, v224, v225\n\t"
    "v_cvt_pk_bf16_f32 v121, v226, v227\n\t"
    "v_mfma_f32_16x16x32_bf16 v[232:235], a[32:35], v[48:51], 0\n\t"
    "v_mfma_f32_16x16x32_bf16 v[232:235], a[36:39], v[52:55], v[232:235]\n\t"
    "v_mfma_f32_16x16x32_bf16 v[232:235], a[40:43], v[56:59], v[232:235]\n\t"
    "v_mfma_f32_16x16x32_bf16 v[232:235], a[44:47], v[60:63], v[232:235]\n\t"
    "buffer_load_dwordx4 v250, s[20:23], s40 offen offset:2176 nt lds\n\t"
    "v_cvt_pk_bf16_f32 v122, v228, v229\n\t"
    "v_mfma_f32_16x16x32_bf16 v[236:239], a[60:63], v[60:63], 0\n\t"
    "v_cvt_pk_bf16_f32 v123, v230, v231\n\t"
    "v_mfma_f32_16x16x32_bf16 v[236:239], a[56:59], v[56:59], v[236:239]\n\t"
    "v_mfma_f32_16x16x32_bf16 v[236:239], a[52:55], v[52:55], v[236:239]\n\t"
    "s_waitcnt vmcnt(7)\n\t"
    "s_barrier\n\t"
    "ds_read_b64_tr_b16 a[128:129], v9 offset:32768\n\t"
    "v_mfma_f32_16x16x32_bf16 v[236:239], a[48:51], v[48:51], v[236:239]\n\t"
    "ds_read_b64_tr_b16 a[130:131], v9 offset:33024\n\t"
    "v_cvt_pk_bf16_f32 v124, v232, v233\n\t"
    "v_mfma_f32_16x16x32_bf16 v[216:219], a[64:67], v[48:51], 0\n\t"
    "ds_read_b64_tr_b16 a[132:133], v9 offset:36896\n\t"
    "v_cvt_pk_bf16_f32 v125, v234, v235\n\t"
    "v_mfma_f32_16x16x32_bf16 v[216:219], a[68:71], v[52:55], v[216:219]\n\t"
    "ds_read_b64_tr_b16 a[134:135], v9 offset:37152\n\t"
    "v_mfma_f32_16x16x32_bf16 v[216:219], a[72:75], v[56:59], v[216:219]\n\t"
    "ds_read_b64_tr_b16 a[136:137], v9 offset:41024\n\t"
    "buffer_load_dwordx4 v251, s[20:23], s40 offen offset:3264 nt lds\n\t"
    "v_mfma_f32_16x16x32_bf16 v[216:219], a[76:79], v[60:63], v[216:219]\n\t"
    "ds_read_b64_tr_b16 a[138:139], v9 offset:41280\n\t"
    "s_add_u32 m0, s33, 13056\n\t"
    "v_mfma_f32_16x16x32_bf16 v[220:223], a[92:95], v[60:63], 0\n\t"
    "ds_read_b64_tr_b16 a[140:141], v9 offset:45152\n\t"
    "v_cvt_pk_bf16_f32 v126, v236, v237\n\t"
    "v_mfma_f32_16x16x32_bf16 v[220:223], a[88:91], v[56:59], v[220:223]\n\t"
    "ds_read_b64_tr_b16 a[142:143], v9 offset:45408\n\t"
    "v_cvt_pk_bf16_f32 v127, v238, v239\n\t"
    "v_mfma_f32_16x16x32_bf16 v[220:223], a[84:87], v[52:55], v[220:223]\n\t"
    "ds_read_b64_tr_b16 a[144:145], v9 offset:32776\n\t"
    "v_mfma_f32_16x16x32_bf16 v[220:223], a[80:83], v[48:51], v[220:223]\n\t"
    "ds_read_b64_tr_b16 a[146:147], v9 offset:33032\n\t"
    "v_cvt_pk_bf16_f32 v144, v216, v217\n\t"
    "v_mfma_f32_16x16x32_bf16 v[224:227], a[96:99], v[48:51], 0\n\t"
    "ds_read_b64_tr_b16 a[148:149], v9 offset:36904\n\t"
    "v_cvt_pk_bf16_f32 v145, v218, v219\n\t"
    "v_mfma_f32_16x16x32_bf16 v[224:227], a[100:103], v[52:55], v[224:227]\n\t"
    "ds_read_b64_tr_b16 a[150:151], v9 offset:37160\n\t"
    "buffer_load_dwordx4 v248, s[20:23], s41 offen offset:0 nt lds\n\t"
    "v_mfma_f32_16x16x32_bf16 v[224:227], a[104:107], v[56:59], v[224:227]\n\t"
    "ds_read_b64_tr_b16 a[152:153], v9 offset:41032\n\t"
    "v_mfma_f32_16x16x32_bf16 v[224:227], a[108:111], v[60:63], v[224:227]\n\t"
    "ds_read_b64_tr_b16 a[154:155], v9 offset:41288\n\t"
    "v_cvt_pk_bf16_f32 v146, v220, v221\n\t"
    "v_mfma_f32_16x16x32_bf16 v[228:231], a[124:127], v[60:63], 0\n\t"
    "ds_read_b64_tr_b16 a[156:157], v9 offset:45160\n\t"
    "v_cvt_pk_bf16_f32 v147, v222, v223\n\t"
    "v_mfma_f32_16x16x32_bf16 v[228:231], a[120:123], v[56:59], v[228:231]\n\t"
    "ds_read_b64_tr_b16 a[158:159], v9 offset:45416\n\t"
    "v_mfma_f32_16x16x32_bf16 v[228:231], a[116:119], v[52:55], v[228:231]\n\t"
    "ds_read_b64_tr_b16 a[160:161], v10 offset:32768\n\t"
    "v_mfma_f32_16x16x32_bf16 v[228:231], a[112:115], v[48:51], v[228:231]\n\t"
    "ds_read_b64_tr_b16 a[162:163], v10 offset:33024\n\t"
    "v_cvt_pk_bf16_f32 v148, v224, v225\n\t"
    "s_waitcnt lgkmcnt(15)\n\t"
    "v_mfma_f32_16x16x32_bf16 v[232:235], v[32:35], a[128:131], 0\n\t"
    "ds_read_b64_tr_b16 a[164:165], v10 offset:36896\n\t"
    "buffer_load_dwordx4 v249, s[20:23], s42 offen offset:1088 nt lds\n\t"
    "s_waitcnt lgkmcnt(15)\n\t"
    "v_mfma_f32_16x16x32_bf16 v[232:235], v[36:39], a[132:135], v[232:235]\n\t"
    "ds_read_b64_tr_b16 a[166:167], v10 offset:37152\n\t"
    "v_cvt_pk_bf16_f32 v149, v226, v227\n\t"
    "s_waitcnt lgkmcnt(14)\n\t"
    "v_mfma_f32_16x16x32_bf16 v[232:235], v[40:43], a[136:139], v[232:235]\n\t"
    "ds_read_b64_tr_b16 a[168:169], v10 offset:41024\n\t"
    "s_waitcnt lgkmcnt(13)\n\t"
    "v_mfma_f32_16x16x32_bf16 v[232:235], v[44:47], a[140:143], v[232:235]\n\t"
    "ds_read_b64_tr_b16 a[170:171], v10 offset:41280\n\t"
    "v_cvt_pk_bf16_f32 v150, v228, v229\n\t"
    "v_mfma_f32_16x16x32_bf16 v[236:239], v[60:63], a[140:143], 0\n\t"
    "ds_read_b64_tr_b16 a[172:173], v10 offset:45152\n\t"
    "v_cvt_pk_bf16_f32 v151, v230, v231\n\t"
    "v_mfma_f32_16x16x32_bf16 v[236:239], v[56:59], a[136:139], v[236:239]\n\t"
    "ds_read_b64_tr_b16 a[174:175], v10 offset:45408\n\t"
    "v_mfma_f32_16x16x32_bf16 v[236:239], v[52:55], a[132:135], v[236:239]\n\t"
    "ds_read_b64_tr_b16 a[176:177], v10 offset:32776\n\t"
    "v_mfma_f32_16x16x32_bf16 v[236:239], v[48:51], a[128:131], v[236:239]\n\t"
    "ds_read_b64_tr_b16 a[178:179], v10 offset:33032\n\t"
    "buffer_load_dwordx4 v250, s[20:23], s42 offen offset:2176 nt lds\n\t"
    "v_mfma_f32_16x16x32_bf16 v[192:195], v[136:139], v[112:115], 0\n\t"
    "ds_read_b64_tr_b16 a[180:181], v10 offset:36904\n\t"
    "v_cvt_pk_bf16_f32 v160, v232, v233\n\t"
    "v_mfma_f32_16x16x32_bf16 v[200:203], v[136:139], v[120:123], 0\n\t"
    "ds_read_b64_tr_b16 a[182:183], v10 offset:37160\n\t"
    "v_cvt_pk_bf16_f32 v161, v234, v235\n\t"
    "v_mfma_f32_16x16x32_bf16 v[204:207], v[144:147], v[120:123], v[212:215]\n\t"
    "ds_read_b64_tr_b16 a[184:185], v10 offset:41032\n\t"
    "v_mfma_f32_16x16x32_bf16 v[196:199], v[144:147], v[112:115], v[212:215]\n\t"
    "ds_read_b64_tr_b16 a[186:187], v10 offset:41288\n\t"
    "v_cvt_pk_bf16_f32 v162, v236, v237\n\t"
    "v_mfma_f32_16x16x32_bf16 v[192:195], v[140:143], v[116:119], v[192:195]\n\t"
    "ds_read_b64_tr_b16 a[188:189], v10 offset:45160\n\t"
    "v_cvt_pk_bf16_f32 v163, v238, v239\n\t"
    "v_mfma_f32_16x16x32_bf16 v[200:203], v[140:143], v[124:127], v[200:203]\n\t"
    "ds_read_b64_tr_b16 a[190:191], v10 offset:45416\n\t"
    "v_mfma_f32_16x16x32_bf16 v[204:207], v[148:151], v[124:127], v[204:207]\n\t"
    "buffer_load_dwordx4 v251, s[20:23], s42 offen offset:3264 nt lds\n\t"
    "v_mfma_f32_16x16x32_bf16 v[196:199], v[148:151], v[116:119], v[196:199]\n\t"
    "s_add_u32 m0, s33, 17408\n\t"
    "s_waitcnt lgkmcnt(15)\n\t"
    "v_mfma_f32_16x16x32_bf16 v[216:219], v[48:51], a[144:147], 0\n\t"
    "ds_read_b128 v[212:215], v13 offset:64\n\t"
    "v_max3_f32 v240, v192, v193, v194\n\t"
    "v_mfma_f32_16x16x32_bf16 v[216:219], v[52:55], a[148:151], v[216:219]\n\t"
    "v_max3_f32 v241, v200, v201, v202\n\t"
    "v_mfma_f32_16x16x32_bf16 v[216:219], v[56:59], a[152:155], v[216:219]\n\t"
    "s_waitcnt vmcnt(8)\n\t"
    "s_waitcnt lgkmcnt(1)\n\t"
    "s_barrier\n\t"
    "s_bitcmp1_b32 s29, 0\n\t"
    "s_cbranch_scc0 .Lg0_%=\n\t"
    "s_nop 7\n\t"
    "s_nop 7\n\t"
    "s_nop 7\n\t"
    "s_nop 7\n\t"
    "s_nop 7\n\t"
    "s_nop 7\n\t"
    "s_nop 7\n\t"
    "s_nop 7\n\t"
    "s_nop 7\n\t"
    "s_nop 7\n\t"
    "s_nop 7\n\t"
    "s_nop 7\n\t"
    "s_nop 3\n\t"
    ".Lg0_%=:\n\t"
    "s_bitcmp1_b32 s29, 1\n\t"
    "s_cbranch_scc0 .Lg1_%=\n\t"
    "s_nop 7\n\t"
    "s_nop 7\n\t"
    "s_nop 7\n\t"
    "s_nop 7\n\t"
    "s_nop 7\n\t"
    "s_nop 7\n\t"
    "s_nop 7\n\t"
    "s_nop 7\n\t"
    "s_nop 7\n\t"
    "s_nop 7\n\t"
    "s_nop 7\n\t"
    "s_nop 7\n\t"
    "s_nop 7\n\t"
    "s_nop 7\n\t"
    "s_nop 7\n\t"
    "s_nop 7\n\t"
    "s_nop 7\n\t"
    "s_nop 7\n\t"
    "s_nop 7\n\t"
    "s_nop 7\n\t"
    "s_nop 7\n\t"
    "s_nop 7\n\t"
    "s_nop 7\n\t"
    "s_nop 7\n\t"
    "s_nop 7\n\t"
    ".Lg1_%=:\n\t"
    "ds_read_b64_tr_b16 a[192:193], v9 offset:49280\n\t"
    "v_mfma_f32_16x16x32_bf16 v[216:219], v[60:63], a[156:159], v[216:219]\n\t"
    "ds_read_b64_tr_b16 a[194:195], v9 offset:49536\n\t"
    "v_max3_f32 v243, v204, v205, v206\n\t"
    "v_mfma_f32_16x16x32_bf16 v[220:223], v[44:47], a[156:159], 0\n\t"
    "ds_read_b64_tr_b16 a[196:197], v9 offset:53408\n\t"
    "v_max3_f32 v241, v241, v203, v207\n\t"
    "v_mfma_f32_16x16x32_bf16 v[220:223], v[40:43], a[152:155], v[220:223]\n\t"
    "ds_read_b64_tr_b16 a[198:199], v9 offset:53664\n\t"
    "buffer_load_dwordx4 v248, s[20:23], s43 offen offset:0 nt lds\n\t"
    "v_mfma_f32_16x16x32_bf16 v[220:223], v[36:39], a[148:151], v[220:223]\n\t"
    "ds_read_b64_tr_b16 a[200:201], v9 offset:57536\n\t"
    "v_max3_f32 v242, v196, v197, v198\n\t"
    "v_mfma_f32_16x16x32_bf16 v[220:223], v[32:35], a[144:147], v[220:223]\n\t"
    "ds_read_b64_tr_b16 a[202:203], v9 offset:57792\n\t"
    "v_max3_f32 v240, v240, v195, v199\n\t"
    "v_mfma_f32_16x16x32_bf16 v[224:227], v[32:35], a[160:163], 0\n\t"
    "ds_read_b64_tr_b16 a[204:205], v9 offset:61664\n\t"
    "v_max_f32 v240, v240, v242\n\t"
    "v_mfma_f32_16x16x32_bf16 v[224:227], v[36:39], a[164:167], v[224:227]\n\t"
    "ds_read_b64_tr_b16 a[206:207], v9 offset:61920\n\t"
    "v_max_f32 v241, v241, v243\n\t"
    "v_mfma_f32_16x16x32_bf16 v[224:227], v[40:43], a[168:171], v[224:227]\n\t"
    "ds_read_b64_tr_b16 a[220:221], v9 offset:61672\n\t"
    "v_cvt_pk_bf16_f32 v166, v216, v217\n\t"
    "v_mfma_f32_16x16x32_bf16 v[224:227], v[44:47], a[172:175], v[224:227]\n\t"
    "ds_read_b64_tr_b16 a[222:223], v9 offset:61928\n\t"
    "v_cvt_pk_bf16_f32 v167, v218, v219\n\t"
    "v_mfma_f32_16x16x32_bf16 v[228:231], v[60:63], a[172:175], 0\n\t"
    "ds_read_b64_tr_b16 a[216:217], v9 offset:57544\n\t"
    "buffer_load_dwordx4 v249, s[20:23], s43 offen offset:1088 nt lds\n\t"
    "v_mfma_f32_16x16x32_bf16 v[228:231], v[56:59], a[168:171], v[228:231]\n\t"
    "ds_read_b64_tr_b16 a[218:219], v9 offset:57800\n\t"
    "v_cvt_pk_bf16_f32 v164, v220, v221\n\t"
    "v_mfma_f32_16x16x32_bf16 v[228:231], v[52:55], a[164:167], v[228:231]\n\t"
    "ds_read_b64_tr_b16 a[212:213], v9 offset:53416\n\t"
    "v_cvt_pk_bf16_f32 v165, v222, v223\n\t"
    "v_mfma_f32_16x16x32_bf16 v[228:231], v[48:51], a[160:163], v[228:231]\n\t"
    "ds_read_b64_tr_b16 a[214:215], v9 offset:53672\n\t"
    "v_cvt_pk_bf16_f32 v168, v224, v225\n\t"
    "v_mfma_f32_16x16x32_bf16 v[232:235], v[48:51], a[176:179], 0\n\t"
    "ds_read_b64_tr_b16 a[208:209], v9 offset:49288\n\t"
    "v_cvt_pk_bf16_f32 v169, v226, v227\n\t"
    "v_mfma_f32_16x16x32_bf16 v[232:235], v[52:55], a[180:183], v[232:235]\n\t"
    "ds_read_b64_tr_b16 a[210:211], v9 offset:49544\n\t"
    "v_mfma_f32_16x16x32_bf16 v[232:235], v[56:59], a[184:187], v[232:235]\n\t"
    "ds_read_b64_tr_b16 a[224:225], v10 offset:49280\n\t"
    "v_mfma_f32_16x16x32_bf16 v[232:235], v[60:63], a[188:191], v[232:235]\n\t"
    "ds_read_b64_tr_b16 a[226:227], v10 offset:49536\n\t"
    "buffer_load_dwordx4 v250, s[20:23], s43 offen offset:2176 nt lds\n\t"
    "v_mfma_f32_16x16x32_bf16 v[236:239], v[44:47], a[188:191], 0\n\t"
    "ds_read_b64_tr_b16 a[228:229], v10 offset:53408\n\t"
    "v_cvt_pk_bf16_f32 v170, v228, v229\n\t"
    "v_mfma_f32_16x16x32_bf16 v[236:239], v[40:43], a[184:187], v[236:239]\n\t"
    "ds_read_b64_tr_b16 a[230:231], v10 offset:53664\n\t"
    "v_cvt_pk_bf16_f32 v171, v230, v231\n\t"
    "v_mfma_f32_16x16x32_bf16 v[236:239], v[36:39], a[180:183], v[236:239]\n\t"
    "ds_read_b64_tr_b16 a[232:233], v10 offset:57536\n\t"
    "v_mfma_f32_16x16x32_bf16 v[236:239], v[32:35], a[176:179], v[236:239]\n\t"
    "ds_read_b64_tr_b16 a[234:235], v10 offset:57792\n\t"
    "v_cvt_pk_bf16_f32 v174, v232, v233\n\t"
    "s_waitcnt lgkmcnt(15)\n\t"
    "v_mfma_f32_16x16x32_bf16 v[64:67], a[192:195], v[32:35], 0\n\t"
    "ds_read_b64_tr_b16 a[236:237], v10 offset:61664\n\t"
    "v_cvt_pk_bf16_f32 v175, v234, v235\n\t"
    "v_mfma_f32_16x16x32_bf16 v[64:67], a[196:199], v[36:39], v[64:67]\n\t"
    "ds_read_b64_tr_b16 a[238:239], v10 offset:61920\n\t"
    "v_mfma_f32_16x16x32_bf16 v[64:67], a[200:203], v[40:43], v[64:67]\n\t"
    "ds_read_b64_tr_b16 a[252:253], v10 offset:61672\n\t"
    "buffer_load_dwordx4 v21, s[20:23], s43 offen offset:3264 nt lds\n\t"
    "s_waitcnt lgkmcnt(15)\n\t"
    "v_mfma_f32_16x16x32_bf16 v[64:67], a[204:207], v[44:47], v[64:67]\n\t"
    "ds_read_b64_tr_b16 a[254:255], v10 offset:61928\n\t"
    "v_cvt_pk_bf16_f32 v172, v236, v237\n\t"
    "v_mfma_f32_16x16x32_bf16 v[68:71], a[220:223], v[44:47], 0\n\t"
    "ds_read_b64_tr_b16 a[248:249], v10 offset:57544\n\t"
    "v_cvt_pk_bf16_f32 v173, v238, v239\n\t"
    "s_waitcnt lgkmcnt(15)\n\t"
    "v_mfma_f32_16x16x32_bf16 v[68:71], a[216:219], v[40:43], v[68:71]\n\t"
    "ds_read_b64_tr_b16 a[250:251], v10 offset:57800\n\t"
    "s_waitcnt lgkmcnt(14)\n\t"
    "v_mfma_f32_16x16x32_bf16 v[68:71], a[212:215], v[36:39], v[68:71]\n\t"
    "ds_read_b64_tr_b16 a[244:245], v10 offset:53416\n\t"
    "s_waitcnt lgkmcnt(13)\n\t"
    "v_mfma_f32_16x16x32_bf16 v[68:71], a[208:211], v[32:35], v[68:71]\n\t"
    "ds_read_b64_tr_b16 a[246:247], v10 offset:53672\n\t"
    "s_waitcnt lgkmcnt(12)\n\t"
    "v_mfma_f32_16x16x32_bf16 v[72:75], a[224:227], v[32:35], 0\n\t"
    "ds_read_b64_tr_b16 a[240:241], v10 offset:49288\n\t"
    "s_waitcnt lgkmcnt(11)\n\t"
    "v_mfma_f32_16x16x32_bf16 v[72:75], a[228:231], v[36:39], v[72:75]\n\t"
    "ds_read_b64_tr_b16 a[242:243], v10 offset:49544\n\t"
    "s_waitcnt lgkmcnt(10)\n\t"
    "v_mfma_f32_16x16x32_bf16 v[72:75], a[232:235], v[40:43], v[72:75]\n\t"
    "v_permlane32_swap_b32 v240, v241\n\t"
    "s_waitcnt lgkmcnt(8)\n\t"
    "v_mfma_f32_16x16x32_bf16 v[72:75], a[236:239], v[44:47], v[72:75]\n\t"
    "s_waitcnt vmcnt(8)\n\t"
    "v_max_f32 v240, v240, v241\n\t"
    "s_waitcnt lgkmcnt(6)\n\t"
    "v_mfma_f32_16x16x32_bf16 v[76:79], a[252:255], v[44:47], 0\n\t"
    "ds_read_b128 v[44:47], v11 offset:8896\n\t"
    "v_mov_b32 v242, v240\n\t"
    "s_waitcnt lgkmcnt(5)\n\t"
    "v_mfma_f32_16x16x32_bf16 v[76:79], a[248:251], v[40:43], v[76:79]\n\t"
    "ds_read_b128 v[40:43], v11 offset:8832\n\t"
    "s_waitcnt lgkmcnt(4)\n\t"
    "v_mfma_f32_16x16x32_bf16 v[76:79], a[244:247], v[36:39], v[76:79]\n\t"
    "ds_read_b128 v[36:39], v11 offset:8768\n\t"
    "s_waitcnt lgkmcnt(3)\n\t"
    "v_mfma_f32_16x16x32_bf16 v[76:79], a[240:243], v[32:35], v[76:79]\n\t"
    "ds_read_b128 v[32:35], v11 offset:8704\n\t"
    "v_mfma_f32_16x16x32_bf16 v[80:83], a[192:195], v[48:51], 0\n\t"
    "s_add_u32 m0, s33, 0\n\t"
    "v_mfma_f32_16x16x32_bf16 v[80:83], a[196:199], v[52:55], v[80:83]\n\t"
    "buffer_load_dwordx4 v248, s[20:23], s45 offen offset:0 nt lds\n\t"
    "v_mfma_f32_16x16x32_bf16 v[80:83], a[200:203], v[56:59], v[80:83]\n\t"
    "v_permlane16_swap_b32 v240, v242\n\t"
    "v_mfma_f32_16x16x32_bf16 v[80:83], a[204:207], v[60:63], v[80:83]\n\t"
    "v_max_f32 v240, v240, v242\n\t"
    "v_mul_f32 v241, 0xbe38aa3b, v240\n\t"
    "v_mfma_f32_16x16x32_bf16 v[84:87], a[220:223], v[60:63], 0\n\t"
    "buffer_load_dwordx4 v249, s[20:23], s45 offen offset:1088 nt lds\n\t"
    "v_mul_f32 v240, 0xbe38aa3b, v240\n\t"
    "v_mfma_f32_16x16x32_bf16 v[84:87], a[216:219], v[56:59], v[84:87]\n\t"
    "s_nop 0\n\t"
    "v_permlane32_swap_b32 v240, v241\n\t"
    "v_mfma_f32_16x16x32_bf16 v[84:87], a[212:215], v[52:55], v[84:87]\n\t"
    "v_fmamk_f32 v192, v192, 0x3e38aa3b, v240\n\t"
    "v_fmamk_f32 v193, v193, 0x3e38aa3b, v240\n\t"
    "v_mfma_f32_16x16x32_bf16 v[84:87], a[208:211], v[48:51], v[84:87]\n\t"
    "buffer_load_dwordx4 v250, s[20:23], s45 offen offset:2176 nt lds\n\t"
    "v_fmamk_f32 v194, v194, 0x3e38aa3b, v240\n\t"
    "v_mfma_f32_16x16x32_bf16 v[88:91], a[224:227], v[48:51], 0\n\t"
    "v_fmamk_f32 v195, v195, 0x3e38aa3b, v240\n\t"
    "v_fmamk_f32 v200, v200, 0x3e38aa3b, v241\n\t"
    "v_mfma_f32_16x16x32_bf16 v[88:91], a[228:231], v[52:55], v[88:91]\n\t"
    "v_fmamk_f32 v201, v201, 0x3e38aa3b, v241\n\t"
    "v_fmamk_f32 v202, v202, 0x3e38aa3b, v241\n\t"
    "v_mfma_f32_16x16x32_bf16 v[88:91], a[232:235], v[56:59], v[88:91]\n\t"
    "buffer_load_dwordx4 v251, s[20:23], s45 offen offset:3264 nt lds\n\t"
    "s_add_u32 m0, s33, 4352\n\t"
    "v_mfma_f32_16x16x32_bf16 v[88:91], a[236:239], v[60:63], v[88:91]\n\t"
    "buffer_load_dwordx4 v248, s[20:23], s46 offen offset:0 nt lds\n\t"
    "v_fmamk_f32 v203, v203, 0x3e38aa3b, v241\n\t"
    "v_mfma_f32_16x16x32_bf16 v[92:95], a[252:255], v[60:63], 0\n\t"
    "s_waitcnt lgkmcnt(0)\n\t"
    "v_fmamk_f32 v196, v196, 0x3e38aa3b, v240\n\t"
    "v_mfma_f32_16x16x32_bf16 v[92:95], a[248:251], v[56:59], v[92:95]\n\t"
    "v_exp_f32 v192, v192\n\t"
    "v_mfma_f32_16x16x32_bf16 v[92:95], a[244:247], v[52:55], v[92:95]\n\t"
    "v_exp_f32 v193, v193\n\t"
    "v_mfma_f32_16x16x32_bf16 v[92:95], a[240:243], v[48:51], v[92:95]\n\t"
    "v_exp_f32 v194, v194\n\t"
    "v_mfma_f32_16x16x32_bf16 v[216:219], a[0:3], v[32:35], 0\n\t"
    "buffer_load_dwordx4 v249, s[20:23], s46 offen offset:1088 nt lds\n\t"
    "v_exp_f32 v195, v195\n\t"
    "v_exp_f32 v200, v200\n\t"
    "v_exp_f32 v201, v201\n\t"
    "v_exp_f32 v202, v202\n\t"
    "v_exp_f32 v203, v203\n\t"
    "v_fmamk_f32 v197, v197, 0x3e38aa3b, v240\n\t"
    "v_fmamk_f32 v204, v204, 0x3e38aa3b, v241\n\t"
    "v_fmamk_f32 v205, v205, 0x3e38aa3b, v241\n\t"
    "v_mfma_f32_16x16x32_bf16 v[216:219], a[4:7], v[36:39], v[216:219]\n\t"
    "v_exp_f32 v196, v196\n\t"
    "v_exp_f32 v197, v197\n\t"
    "v_exp_f32 v204, v204\n\t"
    "v_exp_f32 v205, v205\n\t"
    "v_fmamk_f32 v198, v198, 0x3e38aa3b, v240\n\t"
    "v_fmamk_f32 v199, v199, 0x3e38aa3b, v240\n\t"
    "v_fmamk_f32 v206, v206, 0x3e38aa3b, v241\n\t"
    "v_fmamk_f32 v207, v207, 0x3e38aa3b, v241\n\t"
    "v_add_f32 v244, v192, v193\n\t"
    "v_add_f32 v246, v194, v195\n\t"
    "v_add_f32 v245, v200, v201\n\t"
    "v_add_f32 v247, v202, v203\n\t"
    "v_mfma_f32_16x16x32_bf16 v[216:219], a[8:11], v[40:43], v[216:219]\n\t"
    "v_exp_f32 v198, v198\n\t"
    "v_exp_f32 v199, v199\n\t"
    "v_exp_f32 v206, v206\n\t"
    "v_exp_f32 v207, v207\n\t"
    "v_add_f32 v244, v244, v196\n\t"
    "v_add_f32 v246, v246, v197\n\t"
    "v_add_f32 v245, v245, v204\n\t"
    "v_add_f32 v247, v247, v205\n\t"
    "v_mfma_f32_16x16x32_bf16 v[216:219], a[12:15], v[44:47], v[216:219]\n\t"
    "v_add_f32 v244, v244, v198\n\t"
    "v_add_f32 v246, v246, v199\n\t"
    "v_add_f32 v245, v245, v206\n\t"
    "v_add_f32 v247, v247, v207\n\t"
    "v_mfma_f32_16x16x32_bf16 v[220:223], a[28:31], v[44:47], 0\n\t"
    "buffer_load_dwordx4 v250, s[20:23], s47 offen offset:2176 nt lds\n\t"
    "v_add_f32 v244, v244, v246\n\t"
    "v_add_f32 v245, v245, v247\n\t"
    "v_mfma_f32_16x16x32_bf16 v[220:223], a[24:27], v[40:43], v[220:223]\n\t"
    "s_nop 0\n\t"
    "v_permlane32_swap_b32 v244, v245\n\t"
    "v_mfma_f32_16x16x32_bf16 v[220:223], a[20:23], v[36:39], v[220:223]\n\t"
    "v_add_f32 v244, v244, v245\n\t"
    "v_mov_b32 v246, v244\n\t"
    "v_mfma_f32_16x16x32_bf16 v[220:223], a[16:19], v[32:35], v[220:223]\n\t"
    "s_nop 0\n\t"
    "v_permlane16_swap_b32 v244, v246\n\t"
    "v_mfma_f32_16x16x32_bf16 v[224:227], a[32:35], v[32:35], 0\n\t"
    "buffer_load_dwordx4 v251, s[20:23], s47 offen offset:3264 nt lds\n\t"
    "v_add_f32 v244, v244, v246\n\t"
    "v_mfma_f32_16x16x32_bf16 v[224:227], a[36:39], v[36:39], v[224:227]\n\t"
    "v_rcp_f32 v244, v244\n\t"
    "v_mfma_f32_16x16x32_bf16 v[224:227], a[40:43], v[40:43], v[224:227]\n\t"
    "v_mov_b32 v245, v244\n\t"
    "s_waitcnt vmcnt(12)\n\t"
    "v_mfma_f32_16x16x32_bf16 v[224:227], a[44:47], v[44:47], v[224:227]\n\t"
    "v_permlane32_swap_b32 v244, v245\n\t"
    "v_mfma_f32_16x16x32_bf16 v[228:231], a[60:63], v[44:47], 0\n\t"
    "ds_read_b128 v[60:63], v11 offset:13248\n\t"
    "v_mul_f32 v192, v192, v244\n\t"
    "v_mul_f32 v193, v193, v244\n\t"
    "v_mfma_f32_16x16x32_bf16 v[228:231], a[56:59], v[40:43], v[228:231]\n\t"
    "ds_read_b128 v[56:59], v11 offset:13184\n\t"
    "v_cvt_pk_bf16_f32 v192, v192, v193\n\t"
    "v_mul_f32 v194, v194, v244\n\t"
    "v_mul_f32 v195, v195, v244\n\t"
    "v_cndmask_b32 v245, 0, v245, s[58:59]\n\t"
    "v_cvt_pk_bf16_f32 v193, v194, v195\n\t"
    "v_mul_f32 v196, v196, v244\n\t"
    "v_mul_f32 v197, v197, v244\n\t"
    "v_mul_f32 v200, v200, v245\n\t"
    "v_mul_f32 v201, v201, v245\n\t"
    "v_mfma_f32_16x16x32_bf16 v[228:231], a[52:55], v[36:39], v[228:231]\n\t"
    "ds_read_b128 v[52:55], v11 offset:13120\n\t"
    "v_cvt_pk_bf16_f32 v194, v196, v197\n\t"
    "v_mul_f32 v198, v198, v244\n\t"
    "v_mul_f32 v199, v199, v244\n\t"
    "v_cvt_pk_bf16_f32 v200, v200, v201\n\t"
    "v_mul_f32 v202, v202, v245\n\t"
    "v_mul_f32 v203, v203, v245\n\t"
    "v_cvt_pk_bf16_f32 v195, v198, v199\n\t"
    "v_cvt_pk_bf16_f32 v201, v202, v203\n\t"
    "v_mul_f32 v204, v204, v245\n\t"
    "v_mul_f32 v205, v205, v245\n\t"
    "v_mfma_f32_16x16x32_bf16 v[228:231], a[48:51], v[32:35], v[228:231]\n\t"
    "ds_read_b128 v[48:51], v11 offset:13056\n\t"
    "v_cvt_pk_bf16_f32 v202, v204, v205\n\t"
    "v_mul_f32 v206, v206, v245\n\t"
    "v_mul_f32 v207, v207, v245\n\t"
    "v_cvt_pk_bf16_f32 v203, v206, v207\n\t"
    "v_mfma_f32_16x16x32_bf16 v[64:67], v[160:163], v[192:195], v[64:67]\n\t"
    "s_waitcnt lgkmcnt(0)\n\t"
    "v_cvt_pk_bf16_f32 v128, v216, v217\n\t"
    "v_mfma_f32_16x16x32_bf16 v[80:83], v[160:163], v[200:203], v[80:83]\n\t"
    "v_cvt_pk_bf16_f32 v129, v218, v219\n\t"
    "v_cvt_pk_bf16_f32 v130, v220, v221\n\t"
    "v_mfma_f32_16x16x32_bf16 v[84:87], v[164:167], v[200:203], v[84:87]\n\t"
    "v_cvt_pk_bf16_f32 v131, v222, v223\n\t"
    "v_cvt_pk_bf16_f32 v132, v224, v225\n\t"
    "v_mfma_f32_16x16x32_bf16 v[68:71], v[164:167], v[192:195], v[68:71]\n\t"
    "v_cvt_pk_bf16_f32 v133, v226, v227\n\t"
    "v_cvt_pk_bf16_f32 v134, v228, v229\n\t"
    "v_mfma_f32_16x16x32_bf16 v[72:75], v[168:171], v[192:195], v[72:75]\n\t"
    "v_cvt_pk_bf16_f32 v135, v230, v231\n\t"
    "v_cvt_pk_bf16_f32 v64, v64, v65\n\t"
    "v_mfma_f32_16x16x32_bf16 v[88:91], v[168:171], v[200:203], v[88:91]\n\t"
    "v_cvt_pk_bf16_f32 v65, v66, v67\n\t"
    "v_pk_max_i16 v64, v64, 0\n\t"
    "v_mfma_f32_16x16x32_bf16 v[92:95], v[172:175], v[200:203], v[92:95]\n\t"
    "v_pk_max_i16 v65, v65, 0\n\t"
    "v_mfma_f32_16x16x32_bf16 v[76:79], v[172:175], v[192:195], v[76:79]\n\t"
    "v_cvt_pk_bf16_f32 v66, v68, v69\n\t"
    "v_cvt_pk_bf16_f32 v67, v70, v71\n\t"
    "v_mfma_f32_16x16x32_bf16 v[232:235], a[0:3], v[48:51], 0\n\t"
    "v_cvt_pk_bf16_f32 v72, v72, v73\n\t"
    "v_cvt_pk_bf16_f32 v73, v74, v75\n\t"
    "v_mfma_f32_16x16x32_bf16 v[232:235], a[4:7], v[52:55], v[232:235]\n\t"
    "v_pk_max_i16 v66, v66, 0\n\t"
    "v_pk_max_i16 v67, v67, 0\n\t"
    "v_mfma_f32_16x16x32_bf16 v[232:235], a[8:11], v[56:59], v[232:235]\n\t"
    "v_pk_max_i16 v72, v72, 0\n\t"
    "v_pk_max_i16 v73, v73, 0\n\t"
    "v_mfma_f32_16x16x32_bf16 v[232:235], a[12:15], v[60:63], v[232:235]\n\t"
    "v_cvt_pk_bf16_f32 v74, v76, v77\n\t"
    "v_cvt_pk_bf16_f32 v75, v78, v79\n\t"
    "v_mfma_f32_16x16x32_bf16 v[236:239], a[28:31], v[60:63], 0\n\t"
    "v_pk_max_i16 v74, v74, 0\n\t"
    "v_pk_max_i16 v75, v75, 0\n\t"
    "v_mfma_f32_16x16x32_bf16 v[236:239], a[24:27], v[56:59], v[236:239]\n\t"
    "v_cndmask_b32_dpp v76, v72, v64, vcc row_ror:8 row_mask:0xf bank_mask:0xf\n\t"
    "v_cndmask_b32_dpp v77, v73, v65, vcc row_ror:8 row_mask:0xf bank_mask:0xf\n\t"
    "v_mfma_f32_16x16x32_bf16 v[236:239], a[20:23], v[52:55], v[236:239]\n\t"
    "v_cndmask_b32_dpp v78, v74, v66, vcc row_ror:8 row_mask:0xf bank_mask:0xf\n\t"
    "v_cndmask_b32_dpp v79, v75, v67, vcc row_ror:8 row_mask:0xf bank_mask:0xf\n\t"
    "v_mfma_f32_16x16x32_bf16 v[236:239], a[16:19], v[48:51], v[236:239]\n\t"
    "buffer_store_dwordx4 v[76:79], v12, s[24:27], s37 offen offset:0" STPOL "\n\t"
    "v_mfma_f32_16x16x32_bf16 v[216:219], a[32:35], v[48:51], 0\n\t"
    "v_cvt_pk_bf16_f32 v112, v232, v233\n\t"
    "v_cvt_pk_bf16_f32 v113, v234, v235\n\t"
    "v_mfma_f32_16x16x32_bf16 v[216:219], a[36:39], v[52:55], v[216:219]\n\t"
    "v_mov_b32_dpp v72, v64 row_ror:8 row_mask:0xf bank_mask:0x3\n\t"
    "v_mov_b32_dpp v73, v65 row_ror:8 row_mask:0xf bank_mask:0x3\n\t"
    "v_mfma_f32_16x16x32_bf16 v[216:219], a[40:43], v[56:59], v[216:219]\n\t"
    "v_mov_b32_dpp v74, v66 row_ror:8 row_mask:0xf bank_mask:0x3\n\t"
    "v_mov_b32_dpp v75, v67 row_ror:8 row_mask:0xf bank_mask:0x3\n\t"
    "v_mfma_f32_16x16x32_bf16 v[216:219], a[44:47], v[60:63], v[216:219]\n\t"
    "v_cvt_pk_bf16_f32 v114, v236, v237\n\t"
    "v_cvt_pk_bf16_f32 v115, v238, v239\n\t"
    "v_mfma_f32_16x16x32_bf16 v[220:223], a[60:63], v[60:63], 0\n\t"
    "v_mov_b32_dpp v120, v112 quad_perm:[0,1,2,3] row_mask:0xf bank_mask:0x3\n\t"
    "v_mov_b32_dpp v121, v113 quad_perm:[0,1,2,3] row_mask:0xf bank_mask:0x3\n\t"
    "v_mfma_f32_16x16x32_bf16 v[220:223], a[56:59], v[56:59], v[220:223]\n\t"
    "v_mov_b32_dpp v122, v114 quad_perm:[0,1,2,3] row_mask:0xf bank_mask:0x3\n\t"
    "v_mov_b32_dpp v123, v115 quad_perm:[0,1,2,3] row_mask:0xf bank_mask:0x3\n\t"
    "v_mfma_f32_16x16x32_bf16 v[220:223], a[52:55], v[52:55], v[220:223]\n\t"
    "v_mfma_f32_16x16x32_bf16 v[220:223], a[48:51], v[48:51], v[220:223]\n\t"
    "v_cvt_pk_bf16_f32 v116, v216, v217\n\t"
    "v_cvt_pk_bf16_f32 v117, v218, v219\n\t"
    "v_mfma_f32_16x16x32_bf16 v[224:227], a[64:67], v[48:51], 0\n\t"
    "v_mov_b32_dpp v124, v116 quad_perm:[0,1,2,3] row_mask:0xf bank_mask:0x3\n\t"
    "v_mov_b32_dpp v125, v117 quad_perm:[0,1,2,3] row_mask:0xf bank_mask:0x3\n\t"
    "v_mfma_f32_16x16x32_bf16 v[224:227], a[68:71], v[52:55], v[224:227]\n\t"
    "v_mfma_f32_16x16x32_bf16 v[224:227], a[72:75], v[56:59], v[224:227]\n\t"
    "v_mfma_f32_16x16x32_bf16 v[224:227], a[76:79], v[60:63], v[224:227]\n\t"
    "buffer_store_dwordx4 v[72:75], v12, s[24:27], s37 offen offset:2048" STPOL "\n\t"
    "v_mfma_f32_16x16x32_bf16 v[228:231], a[76:79], v[44:47], 0\n\t"
    "v_cvt_pk_bf16_f32 v118, v220, v221\n\t"
    "v_cvt_pk_bf16_f32 v119, v222, v223\n\t"
    "v_mfma_f32_16x16x32_bf16 v[228:231], a[72:75], v[40:43], v[228:231]\n\t"
    "v_mov_b32_dpp v126, v118 quad_perm:[0,1,2,3] row_mask:0xf bank_mask:0x3\n\t"
    "v_mov_b32_dpp v127, v119 quad_perm:[0,1,2,3] row_mask:0xf bank_mask:0x3\n\t"
    "v_mfma_f32_16x16x32_bf16 v[228:231], a[68:71], v[36:39], v[228:231]\n\t"
    "v_mfma_f32_16x16x32_bf16 v[228:231], a[64:67], v[32:35], v[228:231]\n\t"
    "v_cvt_pk_bf16_f32 v136, v224, v225\n\t"
    "v_cvt_pk_bf16_f32 v137, v226, v227\n\t"
    "v_mfma_f32_16x16x32_bf16 v[232:235], a[80:83], v[32:35], 0\n\t"
    "v_mov_b32_dpp v144, v136 quad_perm:[0,1,2,3] row_mask:0xf bank_mask:0x3\n\t"
    "v_mov_b32_dpp v145, v137 quad_perm:[0,1,2,3] row_mask:0xf bank_mask:0x3\n\t"
    "v_mfma_f32_16x16x32_bf16 v[232:235], a[84:87], v[36:39], v[232:235]\n\t"
    "v_mfma_f32_16x16x32_bf16 v[232:235], a[88:91], v[40:43], v[232:235]\n\t"
    "v_mfma_f32_16x16x32_bf16 v[232:235], a[92:95], v[44:47], v[232:235]\n\t"
    "v_cvt_pk_bf16_f32 v152, v228, v229\n\t"
    "v_cvt_pk_bf16_f32 v153, v230, v231\n\t"
    "v_mfma_f32_16x16x32_bf16 v[236:239], a[92:95], v[60:63], 0\n\t"
    "s_add_u32 m0, s34, 0\n\t"
    "v_mfma_f32_16x16x32_bf16 v[236:239], a[88:91], v[56:59], v[236:239]\n\t"
    "buffer_load_dwordx4 v248, s[20:23], s48 offen offset:0 nt lds\n\t"
    "v_mfma_f32_16x16x32_bf16 v[236:239], a[84:87], v[52:55], v[236:239]\n\t"
    "v_mfma_f32_16x16x32_bf16 v[236:239], a[80:83], v[48:51], v[236:239]\n\t"
    "v_cvt_pk_bf16_f32 v154, v232, v233\n\t"
    "v_cvt_pk_bf16_f32 v155, v234, v235\n\t"
    "v_mfma_f32_16x16x32_bf16 v[216:219], a[96:99], v[48:51], 0\n\t"
    "buffer_load_dwordx4 v249, s[20:23], s48 offen offset:1088 nt lds\n\t"
    "v_mfma_f32_16x16x32_bf16 v[216:219], a[100:103], v[52:55], v[216:219]\n\t"
    "v_mfma_f32_16x16x32_bf16 v[216:219], a[104:107], v[56:59], v[216:219]\n\t"
    "v_mfma_f32_16x16x32_bf16 v[216:219], a[108:111], v[60:63], v[216:219]\n\t"
    "buffer_load_dwordx4 v250, s[20:23], s48 offen offset:2176 nt lds\n\t"
    "v_cvt_pk_bf16_f32 v138, v236, v237\n\t"
    "v_mfma_f32_16x16x32_bf16 v[220:223], a[108:111], v[44:47], 0\n\t"
    "v_cvt_pk_bf16_f32 v139, v238, v239\n\t"
    "v_mov_b32_dpp v146, v138 quad_perm:[0,1,2,3] row_mask:0xf bank_mask:0x3\n\t"
    "v_mfma_f32_16x16x32_bf16 v[220:223], a[104:107], v[40:43], v[220:223]\n\t"
    "v_mov_b32_dpp v147, v139 quad_perm:[0,1,2,3] row_mask:0xf bank_mask:0x3\n\t"
    "v_mfma_f32_16x16x32_bf16 v[220:223], a[100:103], v[36:39], v[220:223]\n\t"
    "buffer_load_dwordx4 v251, s[20:23], s48 offen offset:3264 nt lds\n\t"
    "v_mfma_f32_16x16x32_bf16 v[220:223], a[96:99], v[32:35], v[220:223]\n\t"
    "v_cvt_pk_bf16_f32 v140, v216, v217\n\t"
    "v_cvt_pk_bf16_f32 v141, v218, v219\n\t"
    "v_mfma_f32_16x16x32_bf16 v[224:227], a[112:115], v[32:35], 0\n\t"
    ".Ls0_%=:\n\t"
    "s_cbranch_execz .Ls4_%=\n\t"
    "v_mov_b32_dpp v148, v140 quad_perm:[0,1,2,3] row_mask:0xf bank_mask:0x3\n\t"
    "v_mfma_f32_16x16x32_bf16 v[224:227], a[116:119], v[36:39], v[224:227]\n\t"
    "v_mov_b32_dpp v149, v141 quad_perm:[0,1,2,3] row_mask:0xf bank_mask:0x3\n\t"
    "v_mfma_f32_16x16x32_bf16 v[224:227], a[120:123], v[40:43], v[224:227]\n\t"
    "v_mfma_f32_16x16x32_bf16 v[224:227], a[124:127], v[44:47], v[224:227]\n\t"
    "v_cvt_pk_bf16_f32 v156, v220, v221\n\t"
    "v_cvt_pk_bf16_f32 v157, v222, v223\n\t"
    "v_mfma_f32_16x16x32_bf16 v[228:231], a[124:127], v[60:63], 0\n\t"
    "v_mfma_f32_16x16x32_bf16 v[228:231], a[120:123], v[56:59], v[228:231]\n\t"
    "v_mfma_f32_16x16x32_bf16 v[228:231], a[116:119], v[52:55], v[228:231]\n\t"
    "v_mfma_f32_16x16x32_bf16 v[228:231], a[112:115], v[48:51], v[228:231]\n\t"
    "v_cvt_pk_bf16_f32 v158, v224, v225\n\t"
    "v_cvt_pk_bf16_f32 v159, v226, v227\n\t"
    "v_mfma_f32_16x16x32_bf16 v[232:235], v[32:35], a[128:131], 0\n\t"
    "v_mfma_f32_16x16x32_bf16 v[232:235], v[36:39], a[132:135], v[232:235]\n\t"
    "v_mfma_f32_16x16x32_bf16 v[232:235], v[40:43], a[136:139], v[232:235]\n\t"
    "v_mfma_f32_16x16x32_bf16 v[232:235], v[44:47], a[140:143], v[232:235]\n\t"
    "v_cvt_pk_bf16_f32 v142, v228, v229\n\t"
    "v_cvt_pk_bf16_f32 v143, v230, v231\n\t"
    "v_mfma_f32_16x16x32_bf16 v[236:239], v[60:63], a[140:143], 0\n\t"
    "v_mov_b32_dpp v150, v142 quad_perm:[0,1,2,3] row_mask:0xf bank_mask:0x3\n\t"
    "v_mov_b32_dpp v151, v143 quad_perm:[0,1,2,3] row_mask:0xf bank_mask:0x3\n\t"
    "v_mfma_f32_16x16x32_bf16 v[236:239], v[56:59], a[136:139], v[236:239]\n\t"
    "v_mfma_f32_16x16x32_bf16 v[236:239], v[52:55], a[132:135], v[236:239]\n\t"
    "v_mfma_f32_16x16x32_bf16 v[236:239], v[48:51], a[128:131], v[236:239]\n\t"
    "v_cvt_pk_bf16_f32 v160, v232, v233\n\t"
    "v_cvt_pk_bf16_f32 v161, v234, v235\n\t"
    "v_mfma_f32_16x16x32_bf16 v[192:195], v[152:155], v[120:123], 0\n\t"
    "s_add_u32 m0, s34, 17408\n\t"
    "v_mfma_f32_16x16x32_bf16 v[200:203], v[152:155], v[128:131], 0\n\t"
    "buffer_load_dwordx4 v18, s[20:23], s49 offen offset:0 nt lds\n\t"
    "v_mfma_f32_16x16x32_bf16 v[204:207], v[144:147], v[128:131], v[212:215]\n\t"
    "v_mfma_f32_16x16x32_bf16 v[196:199], v[144:147], v[120:123], v[212:215]\n\t"
    "v_cvt_pk_bf16_f32 v176, v236, v237\n\t"
    "v_cvt_pk_bf16_f32 v177, v238, v239\n\t"
    "v_mfma_f32_16x16x32_bf16 v[192:195], v[156:159], v[124:127], v[192:195]\n\t"
    "buffer_load_dwordx4 v249, s[20:23], s66 offen offset:1088 nt lds\n\t"
    "v_mov_b32_dpp v162, v176 quad_perm:[0,1,2,3] row_mask:0x3 bank_mask:0xf\n\t"
    "v_mfma_f32_16x16x32_bf16 v[200:203], v[156:159], v[132:135], v[200:203]\n\t"
    "v_mov_b32_dpp v163, v177 quad_perm:[0,1,2,3] row_mask:0x3 bank_mask:0xf\n\t"
    "v_mfma_f32_16x16x32_bf16 v[204:207], v[148:151], v[132:135], v[204:207]\n\t"
    "v_mfma_f32_16x16x32_bf16 v[196:199], v[148:151], v[124:127], v[196:199]\n\t"
    "buffer_load_dwordx4 v250, s[20:23], s66 offen offset:2176 nt lds\n\t"
    "v_mfma_f32_16x16x32_bf16 v[216:219], v[48:51], a[144:147], 0\n\t"
    "ds_read_b128 v[208:211], v13 offset:128\n\t"
    "ds_read_b128 v[212:215], v13 offset:192\n\t"
    "v_mfma_f32_16x16x32_bf16 v[216:219], v[52:55], a[148:151], v[216:219]\n\t"
    "v_max3_f32 v240, v192, v193, v194\n\t"
    "v_max3_f32 v241, v200, v201, v202\n\t"
    "v_mfma_f32_16x16x32_bf16 v[216:219], v[56:59], a[152:155], v[216:219]\n\t"
    "buffer_load_dwordx4 v251, s[20:23], s66 offen offset:3264 nt lds\n\t"
    "v_max3_f32 v243, v204, v205, v206\n\t"
    "v_mfma_f32_16x16x32_bf16 v[216:219], v[60:63], a[156:159], v[216:219]\n\t"
    "v_max3_f32 v241, v241, v203, v207\n\t"
    "v_max3_f32 v242, v196, v197, v198\n\t"
    "v_mfma_f32_16x16x32_bf16 v[220:223], v[44:47], a[156:159], 0\n\t"
    "v_max3_f32 v240, v240, v195, v199\n\t"
    "v_max_f32 v240, v240, v242\n\t"
    "v_mfma_f32_16x16x32_bf16 v[220:223], v[40:43], a[152:155], v[220:223]\n\t"
    "v_max_f32 v241, v241, v243\n\t"
    "v_mfma_f32_16x16x32_bf16 v[220:223], v[36:39], a[148:151], v[220:223]\n\t"
    "s_nop 0\n\t"
    "v_permlane32_swap_b32 v240, v241\n\t"
    "v_mfma_f32_16x16x32_bf16 v[220:223], v[32:35], a[144:147], v[220:223]\n\t"
    "v_max_f32 v240, v240, v241\n\t"
    "v_mov_b32 v242, v240\n\t"
    "v_mfma_f32_16x16x32_bf16 v[224:227], v[32:35], a[160:163], 0\n\t"
    "s_nop 0\n\t"
    "v_permlane16_swap_b32 v240, v242\n\t"
    "v_mfma_f32_16x16x32_bf16 v[224:227], v[36:39], a[164:167], v[224:227]\n\t"
    "v_max_f32 v240, v240, v242\n\t"
    "v_mul_f32 v241, 0xbe38aa3b, v240\n\t"
    "v_mfma_f32_16x16x32_bf16 v[224:227], v[40:43], a[168:171], v[224:227]\n\t"
    "v_mul_f32 v240, 0xbe38aa3b, v240\n\t"
    "v_cvt_pk_bf16_f32 v180, v216, v217\n\t"
    "v_mfma_f32_16x16x32_bf16 v[224:227], v[44:47], a[172:175], v[224:227]\n\t"
    "v_permlane32_swap_b32 v240, v241\n\t"
    "v_mfma_f32_16x16x32_bf16 v[228:231], v[60:63], a[172:175], 0\n\t"
    "v_fmamk_f32 v192, v192, 0x3e38aa3b, v240\n\t"
    "v_fmamk_f32 v193, v193, 0x3e38aa3b, v240\n\t"
    "v_mfma_f32_16x16x32_bf16 v[228:231], v[56:59], a[168:171], v[228:231]\n\t"
    "v_fmamk_f32 v194, v194, 0x3e38aa3b, v240\n\t"
    "v_fmamk_f32 v195, v195, 0x3e38aa3b, v240\n\t"
    "v_mfma_f32_16x16x32_bf16 v[228:231], v[52:55], a[164:167], v[228:231]\n\t"
    "v_fmamk_f32 v200, v200, 0x3e38aa3b, v241\n\t"
    "v_fmamk_f32 v201, v201, 0x3e38aa3b, v241\n\t"
    "v_mfma_f32_16x16x32_bf16 v[228:231], v[48:51], a[160:163], v[228:231]\n\t"
    "v_fmamk_f32 v202, v202, 0x3e38aa3b, v241\n\t"
    "v_fmamk_f32 v203, v203, 0x3e38aa3b, v241\n\t"
    "v_mfma_f32_16x16x32_bf16 v[232:235], v[48:51], a[176:179], 0\n\t"
    "v_exp_f32 v192, v192\n\t"
    "v_mfma_f32_16x16x32_bf16 v[232:235], v[52:55], a[180:183], v[232:235]\n\t"
    "v_exp_f32 v193, v193\n\t"
    "v_mfma_f32_16x16x32_bf16 v[232:235], v[56:59], a[184:187], v[232:235]\n\t"
    "v_exp_f32 v194, v194\n\t"
    "v_mfma_f32_16x16x32_bf16 v[232:235], v[60:63], a[188:191], v[232:235]\n\t"
    "v_exp_f32 v195, v195\n\t"
    "v_mfma_f32_16x16x32_bf16 v[236:239], v[44:47], a[188:191], 0\n\t"
    "v_exp_f32 v200, v200\n\t"
    "v_mfma_f32_16x16x32_bf16 v[236:239], v[40:43], a[184:187], v[236:239]\n\t"
    "v_exp_f32 v201, v201\n\t"
    "v_mfma_f32_16x16x32_bf16 v[236:239], v[36:39], a[180:183], v[236:239]\n\t"
    "v_exp_f32 v202, v202\n\t"
    "v_mfma_f32_16x16x32_bf16 v[236:239], v[32:35], a[176:179], v[236:239]\n\t"
    "v_exp_f32 v203, v203\n\t"
    "v_mfma_f32_16x16x32_bf16 v[96:99], a[192:195], v[32:35], 0\n\t"
    "v_fmamk_f32 v196, v196, 0x3e38aa3b, v240\n\t"
    "v_fmamk_f32 v197, v197, 0x3e38aa3b, v240\n\t"
    "v_mfma_f32_16x16x32_bf16 v[96:99], a[196:199], v[36:39], v[96:99]\n\t"
    "v_fmamk_f32 v204, v204, 0x3e38aa3b, v241\n\t"
    "v_fmamk_f32 v205, v205, 0x3e38aa3b, v241\n\t"
    "v_mfma_f32_16x16x32_bf16 v[96:99], a[200:203], v[40:43], v[96:99]\n\t"
    "v_cvt_pk_bf16_f32 v181, v218, v219\n\t"
    "v_fmamk_f32 v198, v198, 0x3e38aa3b, v240\n\t"
    "v_mfma_f32_16x16x32_bf16 v[96:99], a[204:207], v[44:47], v[96:99]\n\t"
    "v_exp_f32 v196, v196\n\t"
    "v_mfma_f32_16x16x32_bf16 v[100:103], a[220:223], v[44:47], 0\n\t"
    "v_exp_f32 v197, v197\n\t"
    "v_mfma_f32_16x16x32_bf16 v[100:103], a[216:219], v[40:43], v[100:103]\n\t"
    "v_exp_f32 v204, v204\n\t"
    "v_mfma_f32_16x16x32_bf16 v[100:103], a[212:215], v[36:39], v[100:103]\n\t"
    "v_exp_f32 v205, v205\n\t"
    "v_mfma_f32_16x16x32_bf16 v[100:103], a[208:211], v[32:35], v[100:103]\n\t"
    "v_fmamk_f32 v199, v199, 0x3e38aa3b, v240\n\t"
    "v_fmamk_f32 v206, v206, 0x3e38aa3b, v241\n\t"
    "v_mfma_f32_16x16x32_bf16 v[104:107], a[224:227], v[32:35], 0\n\t"
    "v_fmamk_f32 v207, v207, 0x3e38aa3b, v241\n\t"
    "v_add_f32 v244, v192, v193\n\t"
    "v_mfma_f32_16x16x32_bf16 v[104:107], a[228:231], v[36:39], v[104:107]\n\t"
    "v_add_f32 v246, v194, v195\n\t"
    "v_add_f32 v245, v200, v201\n\t"
    "v_mfma_f32_16x16x32_bf16 v[104:107], a[232:235], v[40:43], v[104:107]\n\t"
    "v_add_f32 v247, v202, v203\n\t"
    "v_add_f32 v244, v244, v196\n\t"
    "v_mfma_f32_16x16x32_bf16 v[104:107], a[236:239], v[44:47], v[104:107]\n\t"
    "s_waitcnt vmcnt(18)\n\t"
    "v_add_f32 v246, v246, v197\n\t"
    "v_mfma_f32_16x16x32_bf16 v[108:111], a[252:255], v[44:47], 0\n\t"
    "ds_read_b128 v[44:47], v11 offset:17600\n\t"
    "v_add_f32 v245, v245, v204\n\t"
    "v_mfma_f32_16x16x32_bf16 v[108:111], a[248:251], v[40:43], v[108:111]\n\t"
    "ds_read_b128 v[40:43], v11 offset:17536\n\t"
    "v_add_f32 v247, v247, v205\n\t"
    "v_mfma_f32_16x16x32_bf16 v[108:111], a[244:247], v[36:39], v[108:111]\n\t"
    "ds_read_b128 v[36:39], v11 offset:17472\n\t"
    "v_cvt_pk_bf16_f32 v164, v220, v221\n\t"
    "v_mfma_f32_16x16x32_bf16 v[108:111], a[240:243], v[32:35], v[108:111]\n\t"
    "ds_read_b128 v[32:35], v11 offset:17408\n\t"
    "v_cvt_pk_bf16_f32 v165, v222, v223\n\t"
    "v_mfma_f32_16x16x32_bf16 v[64:67], a[192:195], v[48:51], 0\n\t"
    "v_exp_f32 v198, v198\n\t"
    "v_mfma_f32_16x16x32_bf16 v[64:67], a[196:199], v[52:55], v[64:67]\n\t"
    "v_exp_f32 v199, v199\n\t"
    "v_mfma_f32_16x16x32_bf16 v[64:67], a[200:203], v[56:59], v[64:67]\n\t"
    "v_exp_f32 v206, v206\n\t"
    "v_mfma_f32_16x16x32_bf16 v[64:67], a[204:207], v[60:63], v[64:67]\n\t"
    "v_exp_f32 v207, v207\n\t"
    "v_mfma_f32_16x16x32_bf16 v[68:71], a[220:223], v[60:63], 0\n\t"
    "v_add_f32 v244, v244, v198\n\t"
    "v_add_f32 v246, v246, v199\n\t"
    "v_mfma_f32_16x16x32_bf16 v[68:71], a[216:219], v[56:59], v[68:71]\n\t"
    "v_add_f32 v245, v245, v206\n\t"
    "v_add_f32 v247, v247, v207\n\t"
    "v_mfma_f32_16x16x32_bf16 v[68:71], a[212:215], v[52:55], v[68:71]\n\t"
    "v_add_f32 v244, v244, v246\n\t"
    "v_add_f32 v245, v245, v247\n\t"
    "v_mfma_f32_16x16x32_bf16 v[68:71], a[208:211], v[48:51], v[68:71]\n\t"
    "s_waitcnt lgkmcnt(0)\n\t"
    "v_cvt_pk_bf16_f32 v168, v224, v225\n\t"
    "v_mfma_f32_16x16x32_bf16 v[72:75], a[224:227], v[48:51], 0\n\t"
    "v_permlane32_swap_b32 v244, v245\n\t"
    "v_mfma_f32_16x16x32_bf16 v[72:75], a[228:231], v[52:55], v[72:75]\n\t"
    "v_add_f32 v244, v244, v245\n\t"
    "v_mov_b32 v246, v244\n\t"
    "v_mfma_f32_16x16x32_bf16 v[72:75], a[232:235], v[56:59], v[72:75]\n\t"
    "s_nop 0\n\t"
    "v_permlane16_swap_b32 v244, v246\n\t"
    "v_mfma_f32_16x16x32_bf16 v[72:75], a[236:239], v[60:63], v[72:75]\n\t"
    "v_add_f32 v244, v244, v246\n\t"
    "v_cvt_pk_bf16_f32 v169, v226, v227\n\t"
    "v_mfma_f32_16x16x32_bf16 v[76:79], a[252:255], v[60:63], 0\n\t"
    "v_rcp_f32 v244, v244\n\t"
    "v_mfma_f32_16x16x32_bf16 v[76:79], a[248:251], v[56:59], v[76:79]\n\t"
    "v_mov_b32 v245, v244\n\t"
    "v_cvt_pk_bf16_f32 v184, v228, v229\n\t"
    "v_mfma_f32_16x16x32_bf16 v[76:79], a[244:247], v[52:55], v[76:79]\n\t"
    "v_permlane32_swap_b32 v244, v245\n\t"
    "v_mfma_f32_16x16x32_bf16 v[76:79], a[240:243], v[48:51], v[76:79]\n\t"
    "v_mul_f32 v192, v192, v244\n\t"
    "v_mul_f32 v193, v193, v244\n\t"
    "v_mfma_f32_16x16x32_bf16 v[216:219], a[0:3], v[32:35], 0\n\t"
    "v_cvt_pk_bf16_f32 v185, v230, v231\n\t"
    "v_cvt_pk_bf16_f32 v192, v192, v193\n\t"
    "v_mfma_f32_16x16x32_bf16 v[216:219], a[4:7], v[36:39], v[216:219]\n\t"
    "v_mul_f32 v194, v194, v244\n\t"
    "v_mul_f32 v195, v195, v244\n\t"
    "v_mfma_f32_16x16x32_bf16 v[216:219], a[8:11], v[40:43], v[216:219]\n\t"
    "v_cvt_pk_bf16_f32 v193, v194, v195\n\t"
    "v_mul_f32 v196, v196, v244\n\t"
    "v_mfma_f32_16x16x32_bf16 v[216:219], a[12:15], v[44:47], v[216:219]\n\t"
    "v_mul_f32 v197, v197, v244\n\t"
    "v_cvt_pk_bf16_f32 v194, v196, v197\n\t"
    "v_mfma_f32_16x16x32_bf16 v[220:223], a[28:31], v[44:47], 0\n\t"
    "v_mul_f32 v198, v198, v244\n\t"
    "v_mul_f32 v199, v199, v244\n\t"
    "v_mfma_f32_16x16x32_bf16 v[220:223], a[24:27], v[40:43], v[220:223]\n\t"
    "v_cvt_pk_bf16_f32 v195, v198, v199\n\t"
    "v_cndmask_b32 v196, 0, v192, s[52:53]\n\t"
    "v_mfma_f32_16x16x32_bf16 v[220:223], a[20:23], v[36:39], v[220:223]\n\t"
    "v_cndmask_b32 v197, 0, v193, s[52:53]\n\t"
    "v_cndmask_b32 v198, 0, v194, s[52:53]\n\t"
    "v_mfma_f32_16x16x32_bf16 v[220:223], a[16:19], v[32:35], v[220:223]\n\t"
    "v_cndmask_b32 v199, 0, v195, s[52:53]\n\t"
    "v_cndmask_b32 v192, v192, 0, s[58:59]\n\t"
    "v_mfma_f32_16x16x32_bf16 v[224:227], a[32:35], v[32:35], 0\n\t"
    "v_cndmask_b32 v193, v193, 0, s[58:59]\n\t"
    "v_cndmask_b32 v194, v194, 0, s[58:59]\n\t"
    "v_mfma_f32_16x16x32_bf16 v[224:227], a[36:39], v[36:39], v[224:227]\n\t"
    "v_cndmask_b32 v195, v195, 0, s[58:59]\n\t"
    "v_mul_f32 v200, v200, v245\n\t"
    "v_mfma_f32_16x16x32_bf16 v[224:227], a[40:43], v[40:43], v[224:227]\n\t"
    "v_mul_f32 v201, v201, v245\n\t"
    "v_cvt_pk_bf16_f32 v200, v200, v201\n\t"
    "v_mfma_f32_16x16x32_bf16 v[224:227], a[44:47], v[44:47], v[224:227]\n\t"
    "v_mul_f32 v202, v202, v245\n\t"
    "v_mul_f32 v203, v203, v245\n\t"
    "v_mfma_f32_16x16x32_bf16 v[228:231], a[60:63], v[44:47], 0\n\t"
    "v_cvt_pk_bf16_f32 v201, v202, v203\n\t"
    "v_mul_f32 v204, v204, v245\n\t"
    "v_mfma_f32_16x16x32_bf16 v[228:231], a[56:59], v[40:43], v[228:231]\n\t"
    "v_mul_f32 v205, v205, v245\n\t"
    "v_cvt_pk_bf16_f32 v202, v204, v205\n\t"
    "v_mfma_f32_16x16x32_bf16 v[228:231], a[52:55], v[36:39], v[228:231]\n\t"
    "v_mul_f32 v206, v206, v245\n\t"
    "v_mul_f32 v207, v207, v245\n\t"
    "v_mfma_f32_16x16x32_bf16 v[228:231], a[48:51], v[32:35], v[228:231]\n\t"
    "v_cvt_pk_bf16_f32 v203, v206, v207\n\t"
    "v_mov_b32_dpp v166, v180 quad_perm:[0,1,2,3] row_mask:0x3 bank_mask:0xf\n\t"
    "v_mfma_f32_16x16x32_bf16 v[80:83], v[160:163], v[192:195], v[80:83]\n\t"
    "v_mov_b32_dpp v167, v181 quad_perm:[0,1,2,3] row_mask:0x3 bank_mask:0xf\n\t"
    "v_mov_b32_dpp v170, v184 quad_perm:[0,1,2,3] row_mask:0x3 bank_mask:0xf\n\t"
    "v_mfma_f32_16x16x32_bf16 v[64:67], v[160:163], v[196:199], v[64:67]\n\t"
    "v_mov_b32_dpp v171, v185 quad_perm:[0,1,2,3] row_mask:0x3 bank_mask:0xf\n\t"
    "v_cvt_pk_bf16_f32 v188, v232, v233\n\t"
    "v_mfma_f32_16x16x32_bf16 v[96:99], v[160:163], v[200:203], v[96:99]\n\t"
    "v_cvt_pk_bf16_f32 v189, v234, v235\n\t"
    "v_mov_b32_dpp v174, v188 quad_perm:[0,1,2,3] row_mask:0x3 bank_mask:0xf\n\t"
    "v_mfma_f32_16x16x32_bf16 v[100:103], v[164:167], v[200:203], v[100:103]\n\t"
    "v_mov_b32_dpp v175, v189 quad_perm:[0,1,2,3] row_mask:0x3 bank_mask:0xf\n\t"
    "v_cvt_pk_bf16_f32 v172, v236, v237\n\t"
    "v_mfma_f32_16x16x32_bf16 v[68:71], v[164:167], v[196:199], v[68:71]\n\t"
    "v_cvt_pk_bf16_f32 v173, v238, v239\n\t"
    "v_cvt_pk_bf16_f32 v120, v216, v217\n\t"
    "v_mfma_f32_16x16x32_bf16 v[84:87], v[164:167], v[192:195], v[84:87]\n\t"
    "v_cvt_pk_bf16_f32 v121, v218, v219\n\t"
    "v_cvt_pk_bf16_f32 v122, v220, v221\n\t"
    "v_mfma_f32_16x16x32_bf16 v[88:91], v[168:171], v[192:195], v[88:91]\n\t"
    "v_cvt_pk_bf16_f32 v123, v222, v223\n\t"
    "v_cvt_pk_bf16_f32 v124, v224, v225\n\t"
    "v_mfma_f32_16x16x32_bf16 v[72:75], v[168:171], v[196:199], v[72:75]\n\t"
    "v_cvt_pk_bf16_f32 v125, v226, v227\n\t"
    "v_cvt_pk_bf16_f32 v126, v228, v229\n\t"
    "v_mfma_f32_16x16x32_bf16 v[104:107], v[168:171], v[200:203], v[104:107]\n\t"
    "v_cvt_pk_bf16_f32 v127, v230, v231\n\t"
    "v_cvt_pk_bf16_f32 v96, v96, v97\n\t"
    "v_mfma_f32_16x16x32_bf16 v[108:111], v[172:175], v[200:203], v[108:111]\n\t"
    "v_cvt_pk_bf16_f32 v97, v98, v99\n\t"
    "v_cvt_pk_bf16_f32 v98, v100, v101\n\t"
    "v_mfma_f32_16x16x32_bf16 v[76:79], v[172:175], v[196:199], v[76:79]\n\t"
    "v_cvt_pk_bf16_f32 v99, v102, v103\n\t"
    "v_pk_max_i16 v96, v96, 0\n\t"
    "v_mfma_f32_16x16x32_bf16 v[92:95], v[172:175], v[192:195], v[92:95]\n\t"
    "v_pk_max_i16 v97, v97, 0\n\t"
    "v_pk_max_i16 v98, v98, 0\n\t"
    "v_mfma_f32_16x16x32_bf16 v[232:235], a[64:67], v[32:35], 0\n\t"
    "v_cvt_pk_bf16_f32 v104, v104, v105\n\t"
    "v_cvt_pk_bf16_f32 v105, v106, v107\n\t"
    "v_mfma_f32_16x16x32_bf16 v[232:235], a[68:71], v[36:39], v[232:235]\n\t"
    "v_cvt_pk_bf16_f32 v106, v108, v109\n\t"
    "v_cvt_pk_bf16_f32 v107, v110, v111\n\t"
    "v_mfma_f32_16x16x32_bf16 v[232:235], a[72:75], v[40:43], v[232:235]\n\t"
    "v_pk_max_i16 v99, v99, 0\n\t"
    "v_pk_max_i16 v104, v104, 0\n\t"
    "v_mfma_f32_16x16x32_bf16 v[232:235], a[76:79], v[44:47], v[232:235]\n\t"
    "v_pk_max_i16 v105, v105, 0\n\t"
    "v_pk_max_i16 v106, v106, 0\n\t"
    "v_mfma_f32_16x16x32_bf16 v[236:239], a[92:95], v[44:47], 0\n\t"
    "v_pk_max_i16 v107, v107, 0\n\t"
    "v_cvt_pk_bf16_f32 v80, v80, v81\n\t"
    "v_mfma_f32_16x16x32_bf16 v[236:239], a[88:91], v[40:43], v[236:239]\n\t"
    "v_cvt_pk_bf16_f32 v88, v88, v89\n\t"
    "v_cvt_pk_bf16_f32 v81, v82, v83\n\t"
    "v_mfma_f32_16x16x32_bf16 v[236:239], a[84:87], v[36:39], v[236:239]\n\t"
    "v_cvt_pk_bf16_f32 v89, v90, v91\n\t"
    "v_cndmask_b32_dpp v108, v104, v96, vcc row_ror:8 row_mask:0xf bank_mask:0xf\n\t"
    "v_mfma_f32_16x16x32_bf16 v[236:239], a[80:83], v[32:35], v[236:239]\n\t"
    "v_cvt_pk_bf16_f32 v144, v232, v233\n\t"
    "v_cvt_pk_bf16_f32 v145, v234, v235\n\t"
    "v_mfma_f32_16x16x32_bf16 v[216:219], a[96:99], v[32:35], 0\n\t"
    "v_cndmask_b32_dpp v109, v105, v97, vcc row_ror:8 row_mask:0xf bank_mask:0xf\n\t"
    "v_cndmask_b32_dpp v110, v106, v98, vcc row_ror:8 row_mask:0xf bank_mask:0xf\n\t"
    "v_mfma_f32_16x16x32_bf16 v[216:219], a[100:103], v[36:39], v[216:219]\n\t"
    "v_cndmask_b32_dpp v111, v107, v99, vcc row_ror:8 row_mask:0xf bank_mask:0xf\n\t"
    "v_cvt_pk_bf16_f32 v82, v84, v85\n\t"
    "v_mfma_f32_16x16x32_bf16 v[216:219], a[104:107], v[40:43], v[216:219]\n\t"
    "buffer_store_dwordx4 v[108:111], v12, s[24:27], s40 offen offset:0" STPOL "\n\t"
    "v_mfma_f32_16x16x32_bf16 v[216:219], a[108:111], v[44:47], v[216:219]\n\t"
    "v_cvt_pk_bf16_f32 v146, v236, v237\n\t"
    "v_cvt_pk_bf16_f32 v147, v238, v239\n\t"
    "v_mfma_f32_16x16x32_bf16 v[220:223], a[124:127], v[44:47], 0\n\t"
    "v_cvt_pk_bf16_f32 v90, v92, v93\n\t"
    "v_mov_b32_dpp v104, v96 row_ror:8 row_mask:0xf bank_mask:0x3\n\t"
    "v_mfma_f32_16x16x32_bf16 v[220:223], a[120:123], v[40:43], v[220:223]\n\t"
    "v_mov_b32_dpp v105, v97 row_ror:8 row_mask:0xf bank_mask:0x3\n\t"
    "v_mov_b32_dpp v106, v98 row_ror:8 row_mask:0xf bank_mask:0x3\n\t"
    "v_mfma_f32_16x16x32_bf16 v[220:223], a[116:119], v[36:39], v[220:223]\n\t"
    "v_mov_b32_dpp v107, v99 row_ror:8 row_mask:0xf bank_mask:0x3\n\t"
    "v_cvt_pk_bf16_f32 v83, v86, v87\n\t"
    "v_mfma_f32_16x16x32_bf16 v[220:223], a[112:115], v[32:35], v[220:223]\n\t"
    "v_cvt_pk_bf16_f32 v148, v216, v217\n\t"
    "v_cvt_pk_bf16_f32 v149, v218, v219\n\t"
    "v_mfma_f32_16x16x32_bf16 v[224:227], v[32:35], a[128:131], 0\n\t"
    "v_cvt_pk_bf16_f32 v91, v94, v95\n\t"
    "v_pk_max_i16 v80, v80, 0\n\t"
    "v_mfma_f32_16x16x32_bf16 v[224:227], v[36:39], a[132:135], v[224:227]\n\t"
    "v_pk_max_i16 v81, v81, 0\n\t"
    "v_pk_max_i16 v82, v82, 0\n\t"
    "v_mfma_f32_16x16x32_bf16 v[224:227], v[40:43], a[136:139], v[224:227]\n\t"
    "v_pk_max_i16 v83, v83, 0\n\t"
    "v_pk_max_i16 v88, v88, 0\n\t"
    "v_mfma_f32_16x16x32_bf16 v[224:227], v[44:47], a[140:143], v[224:227]\n\t"
    "v_cvt_pk_bf16_f32 v150, v220, v221\n\t"
    "v_cvt_pk_bf16_f32 v151, v222, v223\n\t"
    "v_mfma_f32_16x16x32_bf16 v[228:231], v[44:47], a[156:159], 0\n\t"
    "v_pk_max_i16 v89, v89, 0\n\t"
    "v_pk_max_i16 v90, v90, 0\n\t"
    "v_mfma_f32_16x16x32_bf16 v[228:231], v[40:43], a[152:155], v[228:231]\n\t"
    "v_pk_max_i16 v91, v91, 0\n\t"
    "v_cndmask_b32_dpp v92, v88, v80, vcc row_ror:8 row_mask:0xf bank_mask:0xf\n\t"
    "v_mfma_f32_16x16x32_bf16 v[228:231], v[36:39], a[148:151], v[228:231]\n\t"
    "buffer_store_dwordx4 v[104:107], v12, s[24:27], s40 offen offset:2048" STPOL "\n\t"
    "v_mfma_f32_16x16x32_bf16 v[228:231], v[32:35], a[144:147], v[228:231]\n\t"
    "v_cndmask_b32_dpp v93, v89, v81, vcc row_ror:8 row_mask:0xf bank_mask:0xf\n\t"
    "v_cndmask_b32_dpp v94, v90, v82, vcc row_ror:8 row_mask:0xf bank_mask:0xf\n\t"
    "v_mfma_f32_16x16x32_bf16 v[192:195], v[136:139], v[112:115], v[208:211]\n\t"
    "v_cndmask_b32_dpp v95, v91, v83, vcc row_ror:8 row_mask:0xf bank_mask:0xf\n\t"
    "v_mov_b32_dpp v88, v80 row_ror:8 row_mask:0xf bank_mask:0x3\n\t"
    "v_mfma_f32_16x16x32_bf16 v[200:203], v[136:139], v[120:123], v[208:211]\n\t"
    "v_mov_b32_dpp v89, v81 row_ror:8 row_mask:0xf bank_mask:0x3\n\t"
    "v_mov_b32_dpp v90, v82 row_ror:8 row_mask:0xf bank_mask:0x3\n\t"
    "v_mfma_f32_16x16x32_bf16 v[204:207], v[144:147], v[120:123], v[212:215]\n\t"
    "v_mov_b32_dpp v91, v83 row_ror:8 row_mask:0xf bank_mask:0x3\n\t"
    "s_waitcnt vmcnt(16)\n\t"
    "v_mfma_f32_16x16x32_bf16 v[196:199], v[144:147], v[112:115], v[212:215]\n\t"
    "ds_read_b128 v[60:63], v11 offset:192\n\t"
    "v_cvt_pk_bf16_f32 v178, v224, v225\n\t"
    "v_mfma_f32_16x16x32_bf16 v[192:195], v[140:143], v[116:119], v[192:195]\n\t"
    "ds_read_b128 v[56:59], v11 offset:128\n\t"
    "v_cvt_pk_bf16_f32 v179, v226, v227\n\t"
    "v_mfma_f32_16x16x32_bf16 v[200:203], v[140:143], v[124:127], v[200:203]\n\t"
    "ds_read_b128 v[52:55], v11 offset:64\n\t"
    "v_cvt_pk_bf16_f32 v182, v228, v229\n\t"
    "v_mfma_f32_16x16x32_bf16 v[204:207], v[148:151], v[124:127], v[204:207]\n\t"
    "ds_read_b128 v[48:51], v11 offset:0\n\t"
    "s_add_u32 m0, s34, 34816\n\t"
    "v_mfma_f32_16x16x32_bf16 v[196:199], v[148:151], v[116:119], v[196:199]\n\t"
    "buffer_load_dwordx4 v248, s[20:23], s67 offen offset:0 nt lds\n\t"
    "s_waitcnt lgkmcnt(0)\n\t"
    "v_mfma_f32_16x16x32_bf16 v[232:235], v[32:35], a[160:163], 0\n\t"
    "ds_read_b128 v[212:215], v13 offset:256\n\t"
    "v_cvt_pk_bf16_f32 v183, v230, v231\n\t"
    "v_mfma_f32_16x16x32_bf16 v[232:235], v[36:39], a[164:167], v[232:235]\n\t"
    "v_max3_f32 v240, v192, v193, v194\n\t"
    "v_max3_f32 v241, v200, v201, v202\n\t"
    "v_mfma_f32_16x16x32_bf16 v[232:235], v[40:43], a[168:171], v[232:235]\n\t"
    "buffer_load_dwordx4 v249, s[20:23], s67 offen offset:1088 nt lds\n\t"
    "v_max3_f32 v243, v204, v205, v206\n\t"
    "v_mfma_f32_16x16x32_bf16 v[232:235], v[44:47], a[172:175], v[232:235]\n\t"
    "v_max3_f32 v241, v241, v203, v207\n\t"
    "v_max3_f32 v242, v196, v197, v198\n\t"
    "v_mfma_f32_16x16x32_bf16 v[236:239], v[44:47], a[188:191], 0\n\t"
    "v_max3_f32 v240, v240, v195, v199\n\t"
    "v_max_f32 v240, v240, v242\n\t"
    "v_mfma_f32_16x16x32_bf16 v[236:239], v[40:43], a[184:187], v[236:239]\n\t"
    "buffer_load_dwordx4 v250, s[20:23], s67 offen offset:2176 nt lds\n\t"
    "v_max_f32 v241, v241, v243\n\t"
    "v_mfma_f32_16x16x32_bf16 v[236:239], v[36:39], a[180:183], v[236:239]\n\t"
    "s_nop 0\n\t"
    "v_permlane32_swap_b32 v240, v241\n\t"
    "v_mfma_f32_16x16x32_bf16 v[236:239], v[32:35], a[176:179], v[236:239]\n\t"
    "v_cvt_pk_bf16_f32 v186, v232, v233\n\t"
    "v_cvt_pk_bf16_f32 v187, v234, v235\n\t"
    "v_mfma_f32_16x16x32_bf16 v[96:99], a[192:195], v[32:35], 0\n\t"
    "buffer_load_dwordx4 v251, s[20:23], s68 offen offset:3264 nt lds\n\t"
    "v_max_f32 v240, v240, v241\n\t"
    "v_mfma_f32_16x16x32_bf16 v[96:99], a[196:199], v[36:39], v[96:99]\n\t"
    "buffer_store_dwordx4 v[92:95], v12, s[24:27], s38 offen offset:0" STPOL "\n\t"
    "v_mfma_f32_16x16x32_bf16 v[96:99], a[200:203], v[40:43], v[96:99]\n\t"
    "v_mov_b32 v242, v240\n\t"
    "v_mfma_f32_16x16x32_bf16 v[96:99], a[204:207], v[44:47], v[96:99]\n\t"
    "v_cvt_pk_bf16_f32 v190, v236, v237\n\t"
    "v_cvt_pk_bf16_f32 v191, v238, v239\n\t"
    "v_mfma_f32_16x16x32_bf16 v[100:103], a[220:223], v[44:47], 0\n\t"
    "v_permlane16_swap_b32 v240, v242\n\t"
    "v_mfma_f32_16x16x32_bf16 v[100:103], a[216:219], v[40:43], v[100:103]\n\t"
    "v_max_f32 v240, v240, v242\n\t"
    "v_mul_f32 v241, 0xbe38aa3b, v240\n\t"
    "v_mfma_f32_16x16x32_bf16 v[100:103], a[212:215], v[36:39], v[100:103]\n\t"
    "v_mul_f32 v240, 0xbe38aa3b, v240\n\t"
    "v_mfma_f32_16x16x32_bf16 v[100:103], a[208:211], v[32:35], v[100:103]\n\t"
    "s_nop 0\n\t"
    "v_permlane32_swap_b32 v240, v241\n\t"
    "v_mfma_f32_16x16x32_bf16 v[104:107], a[224:227], v[32:35], 0\n\t"
    "v_fmamk_f32 v192, v192, 0x3e38aa3b, v240\n\t"
    "v_fmamk_f32 v193, v193, 0x3e38aa3b, v240\n\t"
    "v_mfma_f32_16x16x32_bf16 v[104:107], a[228:231], v[36:39], v[104:107]\n\t"
    "v_fmamk_f32 v194, v194, 0x3e38aa3b, v240\n\t"
    "v_fmamk_f32 v195, v195, 0x3e38aa3b, v240\n\t"
    "v_mfma_f32_16x16x32_bf16 v[104:107], a[232:235], v[40:43], v[104:107]\n\t"
    "v_fmamk_f32 v200, v200, 0x3e38aa3b, v241\n\t"
    "v_fmamk_f32 v201, v201, 0x3e38aa3b, v241\n\t"
    "v_mfma_f32_16x16x32_bf16 v[104:107], a[236:239], v[44:47], v[104:107]\n\t"
    "s_waitcnt vmcnt(17)\n\t"
    "v_fmamk_f32 v202, v202, 0x3e38aa3b, v241\n\t"
    "v_mfma_f32_16x16x32_bf16 v[108:111], a[252:255], v[44:47], 0\n\t"
    "ds_read_b128 v[44:47], v11 offset:4544\n\t"
    "v_fmamk_f32 v203, v203, 0x3e38aa3b, v241\n\t"
    "v_mfma_f32_16x16x32_bf16 v[108:111], a[248:251], v[40:43], v[108:111]\n\t"
    "buffer_store_dwordx4 v[88:91], v15, s[24:27], s38 offen offset:2048" STPOL "\n\t"
    "v_mfma_f32_16x16x32_bf16 v[108:111], a[244:247], v[36:39], v[108:111]\n\t"
    "ds_read_b128 v[40:43], v11 offset:4480\n\t"
    "v_fmamk_f32 v196, v196, 0x3e38aa3b, v240\n\t"
    "v_mfma_f32_16x16x32_bf16 v[108:111], a[240:243], v[32:35], v[108:111]\n\t"
    "ds_read_b128 v[36:39], v11 offset:4416\n\t"
    "v_fmamk_f32 v197, v197, 0x3e38aa3b, v240\n\t"
    "v_mfma_f32_16x16x32_bf16 v[216:219], a[0:3], v[48:51], 0\n\t"
    "ds_read_b128 v[32:35], v11 offset:4352\n\t"
    "v_fmamk_f32 v204, v204, 0x3e38aa3b, v241\n\t"
    "v_mfma_f32_16x16x32_bf16 v[216:219], a[4:7], v[52:55], v[216:219]\n\t"
    "v_exp_f32 v192, v192\n\t"
    "v_mfma_f32_16x16x32_bf16 v[216:219], a[8:11], v[56:59], v[216:219]\n\t"
    "v_exp_f32 v193, v193\n\t"
    "v_mfma_f32_16x16x32_bf16 v[216:219], a[12:15], v[60:63], v[216:219]\n\t"
    "v_exp_f32 v194, v194\n\t"
    "v_mfma_f32_16x16x32_bf16 v[220:223], a[28:31], v[60:63], 0\n\t"
    "v_exp_f32 v195, v195\n\t"
    "v_mfma_f32_16x16x32_bf16 v[220:223], a[24:27], v[56:59], v[220:223]\n\t"
    "v_exp_f32 v200, v200\n\t"
    "v_mfma_f32_16x16x32_bf16 v[220:223], a[20:23], v[52:55], v[220:223]\n\t"
    "v_exp_f32 v201, v201\n\t"
    "v_mfma_f32_16x16x32_bf16 v[220:223], a[16:19], v[48:51], v[220:223]\n\t"
    "s_waitcnt lgkmcnt(0)\n\t"
    "v_cvt_pk_bf16_f32 v128, v216, v217\n\t"
    "v_mfma_f32_16x16x32_bf16 v[224:227], a[32:35], v[48:51], 0\n\t"
    "v_cvt_pk_bf16_f32 v129, v218, v219\n\t"
    "v_fmamk_f32 v205, v205, 0x3e38aa3b, v241\n\t"
    "v_mfma_f32_16x16x32_bf16 v[224:227], a[36:39], v[52:55], v[224:227]\n\t"
    "v_exp_f32 v202, v202\n\t"
    "v_mfma_f32_16x16x32_bf16 v[224:227], a[40:43], v[56:59], v[224:227]\n\t"
    "v_exp_f32 v203, v203\n\t"
    "v_mfma_f32_16x16x32_bf16 v[224:227], a[44:47], v[60:63], v[224:227]\n\t"
    "v_cvt_pk_bf16_f32 v130, v220, v221\n\t"
    "v_cvt_pk_bf16_f32 v131, v222, v223\n\t"
    "v_mfma_f32_16x16x32_bf16 v[228:231], a[60:63], v[60:63], 0\n\t"
    "v_exp_f32 v196, v196\n\t"
    "v_mfma_f32_16x16x32_bf16 v[228:231], a[56:59], v[56:59], v[228:231]\n\t"
    "v_exp_f32 v197, v197\n\t"
    "v_mfma_f32_16x16x32_bf16 v[228:231], a[52:55], v[52:55], v[228:231]\n\t"
    "v_exp_f32 v204, v204\n\t"
    "v_mfma_f32_16x16x32_bf16 v[228:231], a[48:51], v[48:51], v[228:231]\n\t"
    "v_cvt_pk_bf16_f32 v132, v224, v225\n\t"
    "v_cvt_pk_bf16_f32 v133, v226, v227\n\t"
    "v_mfma_f32_16x16x32_bf16 v[232:235], a[0:3], v[32:35], 0\n\t"
    "v_exp_f32 v205, v205\n\t"
    "v_mfma_f32_16x16x32_bf16 v[232:235], a[4:7], v[36:39], v[232:235]\n\t"
    "v_fmamk_f32 v198, v198, 0x3e38aa3b, v240\n\t"
    "v_fmamk_f32 v199, v199, 0x3e38aa3b, v240\n\t"
    "v_mfma_f32_16x16x32_bf16 v[232:235], a[8:11], v[40:43], v[232:235]\n\t"
    "v_fmamk_f32 v206, v206, 0x3e38aa3b, v241\n\t"
    "v_fmamk_f32 v207, v207, 0x3e38aa3b, v241\n\t"
    "v_mfma_f32_16x16x32_bf16 v[232:235], a[12:15], v[44:47], v[232:235]\n\t"
    "v_add_f32 v244, v192, v193\n\t"
    "v_add_f32 v246, v194, v195\n\t"
    "v_mfma_f32_16x16x32_bf16 v[236:239], a[28:31], v[44:47], 0\n\t"
    "v_add_f32 v245, v200, v201\n\t"
    "v_add_f32 v247, v202, v203\n\t"
    "v_mfma_f32_16x16x32_bf16 v[236:239], a[24:27], v[40:43], v[236:239]\n\t"
    "v_exp_f32 v198, v198\n\t"
    "v_mfma_f32_16x16x32_bf16 v[236:239], a[20:23], v[36:39], v[236:239]\n\t"
    "v_exp_f32 v199, v199\n\t"
    "v_mfma_f32_16x16x32_bf16 v[236:239], a[16:19], v[32:35], v[236:239]\n\t"
    "v_exp_f32 v206, v206\n\t"
    "v_mfma_f32_16x16x32_bf16 v[216:219], a[32:35], v[32:35], 0\n\t"
    "v_exp_f32 v207, v207\n\t"
    "v_mfma_f32_16x16x32_bf16 v[216:219], a[36:39], v[36:39], v[216:219]\n\t"
    "v_add_f32 v244, v244, v196\n\t"
    "v_add_f32 v246, v246, v197\n\t"
    "v_mfma_f32_16x16x32_bf16 v[216:219], a[40:43], v[40:43], v[216:219]\n\t"
    "v_add_f32 v245, v245, v204\n\t"
    "v_add_f32 v247, v247, v205\n\t"
    "v_mfma_f32_16x16x32_bf16 v[216:219], a[44:47], v[44:47], v[216:219]\n\t"
    "v_add_f32 v244, v244, v198\n\t"
    "v_add_f32 v246, v246, v199\n\t"
    "v_mfma_f32_16x16x32_bf16 v[220:223], a[60:63], v[44:47], 0\n\t"
    "v_add_f32 v245, v245, v206\n\t"
    "v_add_f32 v247, v247, v207\n\t"
    "v_mfma_f32_16x16x32_bf16 v[220:223], a[56:59], v[40:43], v[220:223]\n\t"
    "v_add_f32 v244, v244, v246\n\t"
    "v_add_f32 v245, v245, v247\n\t"
    "v_mfma_f32_16x16x32_bf16 v[220:223], a[52:55], v[36:39], v[220:223]\n\t"
    "v_cvt_pk_bf16_f32 v134, v228, v229\n\t"
    "v_cvt_pk_bf16_f32 v135, v230, v231\n\t"
    "v_mfma_f32_16x16x32_bf16 v[220:223], a[48:51], v[32:35], v[220:223]\n\t"
    "v_permlane32_swap_b32 v244, v245\n\t"
    "v_mfma_f32_16x16x32_bf16 v[224:227], a[64:67], v[32:35], 0\n\t"
    "v_add_f32 v244, v244, v245\n\t"
    "v_mov_b32 v246, v244\n\t"
    "v_mfma_f32_16x16x32_bf16 v[224:227], a[68:71], v[36:39], v[224:227]\n\t"
    "s_nop 0\n\t"
    "v_permlane16_swap_b32 v244, v246\n\t"
    "v_mfma_f32_16x16x32_bf16 v[224:227], a[72:75], v[40:43], v[224:227]\n\t"
    "v_cvt_pk_bf16_f32 v112, v232, v233\n\t"
    "v_cvt_pk_bf16_f32 v113, v234, v235\n\t"
    "v_mfma_f32_16x16x32_bf16 v[224:227], a[76:79], v[44:47], v[224:227]\n\t"
    "v_add_f32 v244, v244, v246\n\t"
    "v_cvt_pk_bf16_f32 v114, v236, v237\n\t"
    "v_mfma_f32_16x16x32_bf16 v[228:231], a[76:79], v[60:63], 0\n\t"
    "v_rcp_f32 v244, v244\n\t"
    "v_mfma_f32_16x16x32_bf16 v[228:231], a[72:75], v[56:59], v[228:231]\n\t"
    "v_mov_b32 v245, v244\n\t"
    "v_cvt_pk_bf16_f32 v115, v238, v239\n\t"
    "v_mfma_f32_16x16x32_bf16 v[228:231], a[68:71], v[52:55], v[228:231]\n\t"
    "v_permlane32_swap_b32 v244, v245\n\t"
    "v_mfma_f32_16x16x32_bf16 v[228:231], a[64:67], v[48:51], v[228:231]\n\t"
    "v_cndmask_b32 v244, v244, 0, s[52:53]\n\t"
    "v_mul_f32 v192, v192, v244\n\t"
    "v_mfma_f32_16x16x32_bf16 v[232:235], a[80:83], v[48:51], 0\n\t"
    "v_mul_f32 v193, v193, v244\n\t"
    "v_cvt_pk_bf16_f32 v192, v192, v193\n\t"
    "v_mfma_f32_16x16x32_bf16 v[232:235], a[84:87], v[52:55], v[232:235]\n\t"
    "v_mul_f32 v194, v194, v244\n\t"
    "v_mul_f32 v195, v195, v244\n\t"
    "v_mfma_f32_16x16x32_bf16 v[232:235], a[88:91], v[56:59], v[232:235]\n\t"
    "v_cndmask_b32 v245, 0, v245, s[62:63]\n\t"
    "v_cvt_pk_bf16_f32 v193, v194, v195\n\t"
    "v_mfma_f32_16x16x32_bf16 v[232:235], a[92:95], v[60:63], v[232:235]\n\t"
    "v_mul_f32 v196, v196, v244\n\t"
    "v_mul_f32 v197, v197, v244\n\t"
    "v_mfma_f32_16x16x32_bf16 v[236:239], a[92:95], v[44:47], 0\n\t"
    "v_mul_f32 v200, v200, v245\n\t"
    "v_mul_f32 v201, v201, v245\n\t"
    "v_mfma_f32_16x16x32_bf16 v[236:239], a[88:91], v[40:43], v[236:239]\n\t"
    "v_cvt_pk_bf16_f32 v194, v196, v197\n\t"
    "v_mul_f32 v198, v198, v244\n\t"
    "v_mfma_f32_16x16x32_bf16 v[236:239], a[84:87], v[36:39], v[236:239]\n\t"
    "v_mul_f32 v199, v199, v244\n\t"
    "v_cvt_pk_bf16_f32 v200, v200, v201\n\t"
    "v_mul_f32 v202, v202, v245\n\t"
    "v_mul_f32 v203, v203, v245\n\t"
    "v_cvt_pk_bf16_f32 v195, v198, v199\n\t"
    "v_cvt_pk_bf16_f32 v201, v202, v203\n\t"
    "v_mul_f32 v204, v204, v245\n\t"
    "v_mul_f32 v205, v205, v245\n\t"
    "v_mfma_f32_16x16x32_bf16 v[236:239], a[80:83], v[32:35], v[236:239]\n\t"
    "v_cvt_pk_bf16_f32 v202, v204, v205\n\t"
    "v_mul_f32 v206, v206, v245\n\t"
    "v_mul_f32 v207, v207, v245\n\t"
    "v_cvt_pk_bf16_f32 v203, v206, v207\n\t"
    "v_mfma_f32_16x16x32_bf16 v[64:67], v[176:179], v[192:195], v[64:67]\n\t"
    "v_cvt_pk_bf16_f32 v116, v216, v217\n\t"
    "v_cvt_pk_bf16_f32 v117, v218, v219\n\t"
    "v_mfma_f32_16x16x32_bf16 v[96:99], v[176:179], v[200:203], v[96:99]\n\t"
    "v_cvt_pk_bf16_f32 v118, v220, v221\n\t"
    "v_cvt_pk_bf16_f32 v119, v222, v223\n\t"
    "v_mfma_f32_16x16x32_bf16 v[100:103], v[180:183], v[200:203], v[100:103]\n\t"
    "v_cvt_pk_bf16_f32 v136, v224, v225\n\t"
    "v_cvt_pk_bf16_f32 v137, v226, v227\n\t"
    "v_mfma_f32_16x16x32_bf16 v[68:71], v[180:183], v[192:195], v[68:71]\n\t"
    "v_cvt_pk_bf16_f32 v152, v228, v229\n\t"
    "v_cvt_pk_bf16_f32 v153, v230, v231\n\t"
    "v_mfma_f32_16x16x32_bf16 v[72:75], v[184:187], v[192:195], v[72:75]\n\t"
    "v_cvt_pk_bf16_f32 v154, v232, v233\n\t"
    "v_cvt_pk_bf16_f32 v155, v234, v235\n\t"
    "v_mfma_f32_16x16x32_bf16 v[104:107], v[184:187], v[200:203], v[104:107]\n\t"
    "v_cvt_pk_bf16_f32 v138, v236, v237\n\t"
    "v_cvt_pk_bf16_f32 v139, v238, v239\n\t"
    "v_mfma_f32_16x16x32_bf16 v[108:111], v[188:191], v[200:203], v[108:111]\n\t"
    "v_mov_b32_dpp v120, v112 quad_perm:[0,1,2,3] row_mask:0xf bank_mask:0x3\n\t"
    "v_mov_b32_dpp v121, v113 quad_perm:[0,1,2,3] row_mask:0xf bank_mask:0x3\n\t"
    "v_mfma_f32_16x16x32_bf16 v[76:79], v[188:191], v[192:195], v[76:79]\n\t"
    "v_mov_b32_dpp v122, v114 quad_perm:[0,1,2,3] row_mask:0xf bank_mask:0x3\n\t"
    "v_mov_b32_dpp v123, v115 quad_perm:[0,1,2,3] row_mask:0xf bank_mask:0x3\n\t"
    "v_mfma_f32_16x16x32_bf16 v[216:219], a[96:99], v[32:35], 0\n\t"
    ".Ls1_%=:\n\t"
    "s_cbranch_execz .Lret%=\n\t"
    "s_add_u32 m0, s33, 8704\n\t"
    "v_mfma_f32_16x16x32_bf16 v[216:219], a[100:103], v[36:39], v[216:219]\n\t"
    "buffer_load_dwordx4 v248, s[20:23], s69 offen offset:0 nt lds\n\t"
    "v_mov_b32_dpp v144, v136 quad_perm:[0,1,2,3] row_mask:0xf bank_mask:0x3\n\t"
    "v_mfma_f32_16x16x32_bf16 v[216:219], a[104:107], v[40:43], v[216:219]\n\t"
    "v_mov_b32_dpp v145, v137 quad_perm:[0,1,2,3] row_mask:0xf bank_mask:0x3\n\t"
    "v_mov_b32_dpp v146, v138 quad_perm:[0,1,2,3] row_mask:0xf bank_mask:0x3\n\t"
    "v_mfma_f32_16x16x32_bf16 v[216:219], a[108:111], v[44:47], v[216:219]\n\t"
    "v_mov_b32_dpp v147, v139 quad_perm:[0,1,2,3] row_mask:0xf bank_mask:0x3\n\t"
    "v_cvt_pk_bf16_f32 v64, v64, v65\n\t"
    "v_mfma_f32_16x16x32_bf16 v[220:223], a[108:111], v[60:63], 0\n\t"
    "buffer_load_dwordx4 v249, s[20:23], s69 offen offset:1088 nt lds\n\t"
    "v_cvt_pk_bf16_f32 v72, v72, v73\n\t"
    "v_mfma_f32_16x16x32_bf16 v[220:223], a[104:107], v[56:59], v[220:223]\n\t"
    "v_mov_b32_dpp v124, v116 quad_perm:[0,1,2,3] row_mask:0xf bank_mask:0x3\n\t"
    "v_mov_b32_dpp v125, v117 quad_perm:[0,1,2,3] row_mask:0xf bank_mask:0x3\n\t"
    "v_mfma_f32_16x16x32_bf16 v[220:223], a[100:103], v[52:55], v[220:223]\n\t"
    "v_mov_b32_dpp v126, v118 quad_perm:[0,1,2,3] row_mask:0xf bank_mask:0x3\n\t"
    "v_mov_b32_dpp v127, v119 quad_perm:[0,1,2,3] row_mask:0xf bank_mask:0x3\n\t"
    "v_mfma_f32_16x16x32_bf16 v[220:223], a[96:99], v[48:51], v[220:223]\n\t"
    "buffer_load_dwordx4 v250, s[20:23], s69 offen offset:2176 nt lds\n\t"
    "v_cvt_pk_bf16_f32 v140, v216, v217\n\t"
    "v_mfma_f32_16x16x32_bf16 v[224:227], a[112:115], v[48:51], 0\n\t"
    "v_cvt_pk_bf16_f32 v141, v218, v219\n\t"
    "v_mov_b32_dpp v148, v140 quad_perm:[0,1,2,3] row_mask:0xf bank_mask:0x3\n\t"
    "v_mfma_f32_16x16x32_bf16 v[224:227], a[116:119], v[52:55], v[224:227]\n\t"
    "v_mov_b32_dpp v149, v141 quad_perm:[0,1,2,3] row_mask:0xf bank_mask:0x3\n\t"
    "v_cvt_pk_bf16_f32 v65, v66, v67\n\t"
    "v_mfma_f32_16x16x32_bf16 v[224:227], a[120:123], v[56:59], v[224:227]\n\t"
    "buffer_load_dwordx4 v251, s[20:23], s69 offen offset:3264 nt lds\n\t"
    "v_cvt_pk_bf16_f32 v73, v74, v75\n\t"
    "v_mfma_f32_16x16x32_bf16 v[224:227], a[124:127], v[60:63], v[224:227]\n\t"
    "v_cvt_pk_bf16_f32 v156, v220, v221\n\t"
    "v_cvt_pk_bf16_f32 v157, v222, v223\n\t"
    "v_mfma_f32_16x16x32_bf16 v[228:231], a[124:127], v[44:47], 0\n\t"
    "v_cvt_pk_bf16_f32 v66, v68, v69\n\t"
    "v_cvt_pk_bf16_f32 v74, v76, v77\n\t"
    "v_mfma_f32_16x16x32_bf16 v[228:231], a[120:123], v[40:43], v[228:231]\n\t"
    "v_cvt_pk_bf16_f32 v67, v70, v71\n\t"
    "v_cvt_pk_bf16_f32 v75, v78, v79\n\t"
    "v_mfma_f32_16x16x32_bf16 v[228:231], a[116:119], v[36:39], v[228:231]\n\t"
    "v_pk_max_i16 v64, v64, 0\n\t"
    "v_pk_max_i16 v65, v65, 0\n\t"
    "v_mfma_f32_16x16x32_bf16 v[228:231], a[112:115], v[32:35], v[228:231]\n\t"
    "v_cvt_pk_bf16_f32 v158, v224, v225\n\t"
    "v_cvt_pk_bf16_f32 v159, v226, v227\n\t"
    "v_mfma_f32_16x16x32_bf16 v[232:235], v[48:51], a[128:131], 0\n\t"
    "s_add_u32 m0, s33, 13056\n\t"
    "v_pk_max_i16 v66, v66, 0\n\t"
    "v_mfma_f32_16x16x32_bf16 v[232:235], v[52:55], a[132:135], v[232:235]\n\t"
    "buffer_load_dwordx4 v248, s[20:23], s70 offen offset:0 nt lds\n\t"
    "v_pk_max_i16 v67, v67, 0\n\t"
    "v_mfma_f32_16x16x32_bf16 v[232:235], v[56:59], a[136:139], v[232:235]\n\t"
    "v_pk_max_i16 v72, v72, 0\n\t"
    "v_pk_max_i16 v73, v73, 0\n\t"
    "v_mfma_f32_16x16x32_bf16 v[232:235], v[60:63], a[140:143], v[232:235]\n\t"
    "v_cvt_pk_bf16_f32 v142, v228, v229\n\t"
    "v_cvt_pk_bf16_f32 v143, v230, v231\n\t"
    "v_mfma_f32_16x16x32_bf16 v[236:239], v[44:47], a[140:143], 0\n\t"
    "buffer_load_dwordx4 v19, s[20:23], s70 offen offset:1088 nt lds\n\t"
    "v_mov_b32_dpp v150, v142 quad_perm:[0,1,2,3] row_mask:0xf bank_mask:0x3\n\t"
    "v_mfma_f32_16x16x32_bf16 v[236:239], v[40:43], a[136:139], v[236:239]\n\t"
    "v_mov_b32_dpp v151, v143 quad_perm:[0,1,2,3] row_mask:0xf bank_mask:0x3\n\t"
    "v_pk_max_i16 v74, v74, 0\n\t"
    "v_mfma_f32_16x16x32_bf16 v[236:239], v[36:39], a[132:135], v[236:239]\n\t"
    "v_pk_max_i16 v75, v75, 0\n\t"
    "v_cndmask_b32_dpp v76, v72, v64, vcc row_ror:8 row_mask:0xf bank_mask:0xf\n\t"
    "v_mfma_f32_16x16x32_bf16 v[236:239], v[32:35], a[128:131], v[236:239]\n\t"
    "buffer_load_dwordx4 v250, s[20:23], s71 offen offset:2176 nt lds\n\t"
    "v_cndmask_b32_dpp v77, v73, v65, vcc row_ror:8 row_mask:0xf bank_mask:0xf\n\t"
    "v_mfma_f32_16x16x32_bf16 v[192:195], v[152:155], v[120:123], 0\n\t"
    "v_cndmask_b32_dpp v78, v74, v66, vcc row_ror:8 row_mask:0xf bank_mask:0xf\n\t"
    "v_cndmask_b32_dpp v79, v75, v67, vcc row_ror:8 row_mask:0xf bank_mask:0xf\n\t"
    "v_mfma_f32_16x16x32_bf16 v[200:203], v[152:155], v[128:131], 0\n\t"
    "v_mov_b32_dpp v72, v64 row_ror:8 row_mask:0xf bank_mask:0x3\n\t"
    "v_mov_b32_dpp v73, v65 row_ror:8 row_mask:0xf bank_mask:0x3\n\t"
    "v_mfma_f32_16x16x32_bf16 v[204:207], v[144:147], v[128:131], v[212:215]\n\t"
    "v_mov_b32_dpp v74, v66 row_ror:8 row_mask:0xf bank_mask:0x3\n\t"
    "v_mov_b32_dpp v75, v67 row_ror:8 row_mask:0xf bank_mask:0x3\n\t"
    "v_mfma_f32_16x16x32_bf16 v[196:199], v[144:147], v[120:123], v[212:215]\n\t"
    "buffer_load_dwordx4 v251, s[20:23], s71 offen offset:3264 nt lds\n\t"
    "v_cvt_pk_bf16_f32 v176, v232, v233\n\t"
    "v_mfma_f32_16x16x32_bf16 v[192:195], v[156:159], v[124:127], v[192:195]\n\t"
    "buffer_store_dwordx4 v[72:75], v12, s[24:27], s42 offen offset:2048" STPOL "\n\t"
    "v_mfma_f32_16x16x32_bf16 v[200:203], v[156:159], v[132:135], v[200:203]\n\t"
    "v_cvt_pk_bf16_f32 v177, v234, v235\n\t"
    "v_cvt_pk_bf16_f32 v160, v236, v237\n\t"
    "v_mfma_f32_16x16x32_bf16 v[204:207], v[148:151], v[132:135], v[204:207]\n\t"
    "v_cvt_pk_bf16_f32 v161, v238, v239\n\t"
    "v_mov_b32_dpp v178, v160 quad_perm:[0,1,2,3] row_mask:0x3 bank_mask:0xf\n\t"
    "v_mfma_f32_16x16x32_bf16 v[196:199], v[148:151], v[124:127], v[196:199]\n\t"
    "v_mov_b32_dpp v179, v161 quad_perm:[0,1,2,3] row_mask:0x3 bank_mask:0xf\n\t"
    "v_mfma_f32_16x16x32_bf16 v[216:219], v[32:35], a[144:147], 0\n\t"
    "ds_read_b128 v[208:211], v13 offset:320\n\t"
    "v_max3_f32 v240, v192, v193, v194\n\t"
    "v_mfma_f32_16x16x32_bf16 v[216:219], v[36:39], a[148:151], v[216:219]\n\t"
    "v_max3_f32 v241, v200, v201, v202\n\t"
    "v_mfma_f32_16x16x32_bf16 v[216:219], v[40:43], a[152:155], v[216:219]\n\t"
    "v_max3_f32 v243, v204, v205, v206\n\t"
    "v_max3_f32 v241, v241, v203, v207\n\t"
    "v_mfma_f32_16x16x32_bf16 v[216:219], v[44:47], a[156:159], v[216:219]\n\t"
    "v_max3_f32 v242, v196, v197, v198\n\t"
    "v_max3_f32 v240, v240, v195, v199\n\t"
    "v_mfma_f32_16x16x32_bf16 v[220:223], v[60:63], a[156:159], 0\n\t"
    "v_max_f32 v240, v240, v242\n\t"
    "v_max_f32 v241, v241, v243\n\t"
    "v_mfma_f32_16x16x32_bf16 v[220:223], v[56:59], a[152:155], v[220:223]\n\t"
    "s_nop 0\n\t"
    "v_permlane32_swap_b32 v240, v241\n\t"
    "v_mfma_f32_16x16x32_bf16 v[220:223], v[52:55], a[148:151], v[220:223]\n\t"
    "v_max_f32 v240, v240, v241\n\t"
    "v_mov_b32 v242, v240\n\t"
    "v_mfma_f32_16x16x32_bf16 v[220:223], v[48:51], a[144:147], v[220:223]\n\t"
    "s_nop 0\n\t"
    "v_permlane16_swap_b32 v240, v242\n\t"
    "v_mfma_f32_16x16x32_bf16 v[224:227], v[48:51], a[160:163], 0\n\t"
    "buffer_store_dwordx4 v[76:79], v16, s[24:27], s41 offen offset:0" STPOL "\n\t"
    "v_mfma_f32_16x16x32_bf16 v[224:227], v[52:55], a[164:167], v[224:227]\n\t"
    "v_max_f32 v240, v240, v242\n\t"
    "v_mul_f32 v241, 0xbe38aa3b, v240\n\t"
    "v_mfma_f32_16x16x32_bf16 v[224:227], v[56:59], a[168:171], v[224:227]\n\t"
    "v_mul_f32 v240, 0xbe38aa3b, v240\n\t"
    "v_cvt_pk_bf16_f32 v164, v216, v217\n\t"
    "v_mfma_f32_16x16x32_bf16 v[224:227], v[60:63], a[172:175], v[224:227]\n\t"
    "v_permlane32_swap_b32 v240, v241\n\t"
    "v_mfma_f32_16x16x32_bf16 v[228:231], v[44:47], a[172:175], 0\n\t"
    "v_fmamk_f32 v192, v192, 0x3e38aa3b, v240\n\t"
    "v_fmamk_f32 v193, v193, 0x3e38aa3b, v240\n\t"
    "v_mfma_f32_16x16x32_bf16 v[228:231], v[40:43], a[168:171], v[228:231]\n\t"
    "v_fmamk_f32 v194, v194, 0x3e38aa3b, v240\n\t"
    "v_fmamk_f32 v195, v195, 0x3e38aa3b, v240\n\t"
    "v_mfma_f32_16x16x32_bf16 v[228:231], v[36:39], a[164:167], v[228:231]\n\t"
    "v_fmamk_f32 v200, v200, 0x3e38aa3b, v241\n\t"
    "v_fmamk_f32 v201, v201, 0x3e38aa3b, v241\n\t"
    "v_mfma_f32_16x16x32_bf16 v[228:231], v[32:35], a[160:163], v[228:231]\n\t"
    "v_fmamk_f32 v202, v202, 0x3e38aa3b, v241\n\t"
    "v_fmamk_f32 v203, v203, 0x3e38aa3b, v241\n\t"
    "v_mfma_f32_16x16x32_bf16 v[232:235], v[32:35], a[176:179], 0\n\t"
    "v_exp_f32 v192, v192\n\t"
    "v_mfma_f32_16x16x32_bf16 v[232:235], v[36:39], a[180:183], v[232:235]\n\t"
    "v_exp_f32 v193, v193\n\t"
    "v_mfma_f32_16x16x32_bf16 v[232:235], v[40:43], a[184:187], v[232:235]\n\t"
    "v_exp_f32 v194, v194\n\t"
    "v_mfma_f32_16x16x32_bf16 v[232:235], v[44:47], a[188:191], v[232:235]\n\t"
    "v_exp_f32 v195, v195\n\t"
    "v_mfma_f32_16x16x32_bf16 v[236:239], v[60:63], a[188:191], 0\n\t"
    "v_exp_f32 v200, v200\n\t"
    "v_mfma_f32_16x16x32_bf16 v[236:239], v[56:59], a[184:187], v[236:239]\n\t"
    "v_exp_f32 v201, v201\n\t"
    "v_mfma_f32_16x16x32_bf16 v[236:239], v[52:55], a[180:183], v[236:239]\n\t"
    "v_exp_f32 v202, v202\n\t"
    "v_mfma_f32_16x16x32_bf16 v[236:239], v[48:51], a[176:179], v[236:239]\n\t"
    "v_exp_f32 v203, v203\n\t"
    "v_mfma_f32_16x16x32_bf16 v[80:83], a[192:195], v[48:51], 0\n\t"
    "v_fmamk_f32 v196, v196, 0x3e38aa3b, v240\n\t"
    "v_fmamk_f32 v197, v197, 0x3e38aa3b, v240\n\t"
    "v_mfma_f32_16x16x32_bf16 v[80:83], a[196:199], v[52:55], v[80:83]\n\t"
    "v_fmamk_f32 v204, v204, 0x3e38aa3b, v241\n\t"
    "v_fmamk_f32 v205, v205, 0x3e38aa3b, v241\n\t"
    "v_mfma_f32_16x16x32_bf16 v[80:83], a[200:203], v[56:59], v[80:83]\n\t"
    "v_cvt_pk_bf16_f32 v165, v218, v219\n\t"
    "v_fmamk_f32 v198, v198, 0x3e38aa3b, v240\n\t"
    "v_mfma_f32_16x16x32_bf16 v[80:83], a[204:207], v[60:63], v[80:83]\n\t"
    "v_exp_f32 v196, v196\n\t"
    "v_mfma_f32_16x16x32_bf16 v[84:87], a[220:223], v[60:63], 0\n\t"
    "v_exp_f32 v197, v197\n\t"
    "v_mfma_f32_16x16x32_bf16 v[84:87], a[216:219], v[56:59], v[84:87]\n\t"
    "v_exp_f32 v204, v204\n\t"
    "v_mfma_f32_16x16x32_bf16 v[84:87], a[212:215], v[52:55], v[84:87]\n\t"
    "v_exp_f32 v205, v205\n\t"
    "v_mfma_f32_16x16x32_bf16 v[84:87], a[208:211], v[48:51], v[84:87]\n\t"
    "v_fmamk_f32 v199, v199, 0x3e38aa3b, v240\n\t"
    "v_fmamk_f32 v206, v206, 0x3e38aa3b, v241\n\t"
    "v_mfma_f32_16x16x32_bf16 v[88:91], a[224:227], v[48:51], 0\n\t"
    "v_fmamk_f32 v207, v207, 0x3e38aa3b, v241\n\t"
    "v_add_f32 v244, v192, v193\n\t"
    "v_mfma_f32_16x16x32_bf16 v[88:91], a[228:231], v[52:55], v[88:91]\n\t"
    "v_add_f32 v246, v194, v195\n\t"
    "v_add_f32 v245, v200, v201\n\t"
    "v_mfma_f32_16x16x32_bf16 v[88:91], a[232:235], v[56:59], v[88:91]\n\t"
    "v_add_f32 v247, v202, v203\n\t"
    "v_add_f32 v244, v244, v196\n\t"
    "v_mfma_f32_16x16x32_bf16 v[88:91], a[236:239], v[60:63], v[88:91]\n\t"
    "s_waitcnt vmcnt(22)\n\t"
    "v_add_f32 v246, v246, v197\n\t"
    "v_mfma_f32_16x16x32_bf16 v[92:95], a[252:255], v[60:63], 0\n\t"
    "ds_read_b128 v[60:63], v14 offset:192\n\t"
    "v_add_f32 v245, v245, v204\n\t"
    "v_mfma_f32_16x16x32_bf16 v[92:95], a[248:251], v[56:59], v[92:95]\n\t"
    "ds_read_b128 v[56:59], v14 offset:128\n\t"
    "v_add_f32 v247, v247, v205\n\t"
    "v_mfma_f32_16x16x32_bf16 v[92:95], a[244:247], v[52:55], v[92:95]\n\t"
    "ds_read_b128 v[52:55], v14 offset:64\n\t"
    "v_cvt_pk_bf16_f32 v180, v220, v221\n\t"
    "v_mfma_f32_16x16x32_bf16 v[92:95], a[240:243], v[48:51], v[92:95]\n\t"
    "ds_read_b128 v[48:51], v14 offset:0\n\t"
    "v_cvt_pk_bf16_f32 v181, v222, v223\n\t"
    "v_mfma_f32_16x16x32_bf16 v[64:67], a[192:195], v[32:35], 0\n\t"
    "v_exp_f32 v198, v198\n\t"
    "v_mfma_f32_16x16x32_bf16 v[64:67], a[196:199], v[36:39], v[64:67]\n\t"
    "v_exp_f32 v199, v199\n\t"
    "v_mfma_f32_16x16x32_bf16 v[64:67], a[200:203], v[40:43], v[64:67]\n\t"
    "v_exp_f32 v206, v206\n\t"
    "v_mfma_f32_16x16x32_bf16 v[64:67], a[204:207], v[44:47], v[64:67]\n\t"
    "v_exp_f32 v207, v207\n\t"
    "v_mfma_f32_16x16x32_bf16 v[68:71], a[220:223], v[44:47], 0\n\t"
    "v_add_f32 v244, v244, v198\n\t"
    "v_add_f32 v246, v246, v199\n\t"
    "v_mfma_f32_16x16x32_bf16 v[68:71], a[216:219], v[40:43], v[68:71]\n\t"
    "v_add_f32 v245, v245, v206\n\t"
    "v_add_f32 v247, v247, v207\n\t"
    "v_mfma_f32_16x16x32_bf16 v[68:71], a[212:215], v[36:39], v[68:71]\n\t"
    "v_add_f32 v244, v244, v246\n\t"
    "v_add_f32 v245, v245, v247\n\t"
    "v_mfma_f32_16x16x32_bf16 v[68:71], a[208:211], v[32:35], v[68:71]\n\t"
    "s_waitcnt lgkmcnt(0)\n\t"
    "v_cvt_pk_bf16_f32 v184, v224, v225\n\t"
    "v_mfma_f32_16x16x32_bf16 v[72:75], a[224:227], v[32:35], 0\n\t"
    "v_permlane32_swap_b32 v244, v245\n\t"
    "v_mfma_f32_16x16x32_bf16 v[72:75], a[228:231], v[36:39], v[72:75]\n\t"
    "v_add_f32 v244, v244, v245\n\t"
    "v_mov_b32 v246, v244\n\t"
    "v_mfma_f32_16x16x32_bf16 v[72:75], a[232:235], v[40:43], v[72:75]\n\t"
    "s_nop 0\n\t"
    "v_permlane16_swap_b32 v244, v246\n\t"
    "v_mfma_f32_16x16x32_bf16 v[72:75], a[236:239], v[44:47], v[72:75]\n\t"
    "v_add_f32 v244, v244, v246\n\t"
    "v_cvt_pk_bf16_f32 v185, v226, v227\n\t"
    "v_mfma_f32_16x16x32_bf16 v[76:79], a[252:255], v[44:47], 0\n\t"
    "v_rcp_f32 v244, v244\n\t"
    "v_mfma_f32_16x16x32_bf16 v[76:79], a[248:251], v[40:43], v[76:79]\n\t"
    "v_mov_b32 v245, v244\n\t"
    "v_cvt_pk_bf16_f32 v168, v228, v229\n\t"
    "v_mfma_f32_16x16x32_bf16 v[76:79], a[244:247], v[36:39], v[76:79]\n\t"
    "v_permlane32_swap_b32 v244, v245\n\t"
    "v_mfma_f32_16x16x32_bf16 v[76:79], a[240:243], v[32:35], v[76:79]\n\t"
    "v_mul_f32 v192, v192, v244\n\t"
    "v_mul_f32 v193, v193, v244\n\t"
    "v_mfma_f32_16x16x32_bf16 v[216:219], a[0:3], v[48:51], 0\n\t"
    "v_cvt_pk_bf16_f32 v169, v230, v231\n\t"
    "v_cvt_pk_bf16_f32 v192, v192, v193\n\t"
    "v_mfma_f32_16x16x32_bf16 v[216:219], a[4:7], v[52:55], v[216:219]\n\t"
    "v_mul_f32 v194, v194, v244\n\t"
    "v_mul_f32 v195, v195, v244\n\t"
    "v_mfma_f32_16x16x32_bf16 v[216:219], a[8:11], v[56:59], v[216:219]\n\t"
    "v_cvt_pk_bf16_f32 v193, v194, v195\n\t"
    "v_mul_f32 v196, v196, v244\n\t"
    "v_mfma_f32_16x16x32_bf16 v[216:219], a[12:15], v[60:63], v[216:219]\n\t"
    "v_mul_f32 v197, v197, v244\n\t"
    "v_cvt_pk_bf16_f32 v194, v196, v197\n\t"
    "v_mfma_f32_16x16x32_bf16 v[220:223], a[28:31], v[60:63], 0\n\t"
    "v_mul_f32 v198, v198, v244\n\t"
    "v_mul_f32 v199, v199, v244\n\t"
    "v_mfma_f32_16x16x32_bf16 v[220:223], a[24:27], v[56:59], v[220:223]\n\t"
    "v_cvt_pk_bf16_f32 v195, v198, v199\n\t"
    "v_cndmask_b32 v196, 0, v192, s[56:57]\n\t"
    "v_mfma_f32_16x16x32_bf16 v[220:223], a[20:23], v[52:55], v[220:223]\n\t"
    "v_cndmask_b32 v197, 0, v193, s[56:57]\n\t"
    "v_cndmask_b32 v198, 0, v194, s[56:57]\n\t"
    "v_mfma_f32_16x16x32_bf16 v[220:223], a[16:19], v[48:51], v[220:223]\n\t"
    "v_cndmask_b32 v199, 0, v195, s[56:57]\n\t"
    "v_cndmask_b32 v192, v192, 0, s[62:63]\n\t"
    "v_mfma_f32_16x16x32_bf16 v[224:227], a[32:35], v[48:51], 0\n\t"
    "v_cndmask_b32 v193, v193, 0, s[62:63]\n\t"
    "v_cndmask_b32 v194, v194, 0, s[62:63]\n\t"
    "v_mfma_f32_16x16x32_bf16 v[224:227], a[36:39], v[52:55], v[224:227]\n\t"
    "v_cndmask_b32 v195, v195, 0, s[62:63]\n\t"
    "v_mul_f32 v200, v200, v245\n\t"
    "v_mfma_f32_16x16x32_bf16 v[224:227], a[40:43], v[56:59], v[224:227]\n\t"
    "v_mul_f32 v201, v201, v245\n\t"
    "v_cvt_pk_bf16_f32 v200, v200, v201\n\t"
    "v_mfma_f32_16x16x32_bf16 v[224:227], a[44:47], v[60:63], v[224:227]\n\t"
    "v_mul_f32 v202, v202, v245\n\t"
    "v_mul_f32 v203, v203, v245\n\t"
    "v_mfma_f32_16x16x32_bf16 v[228:231], a[60:63], v[60:63], 0\n\t"
    "v_cvt_pk_bf16_f32 v201, v202, v203\n\t"
    "v_mul_f32 v204, v204, v245\n\t"
    "v_mfma_f32_16x16x32_bf16 v[228:231], a[56:59], v[56:59], v[228:231]\n\t"
    "v_mul_f32 v205, v205, v245\n\t"
    "s_waitcnt vmcnt(18)\n\t"
    "v_mfma_f32_16x16x32_bf16 v[228:231], a[52:55], v[52:55], v[228:231]\n\t"
    "v_cvt_pk_bf16_f32 v202, v204, v205\n\t"
    "v_mul_f32 v206, v206, v245\n\t"
    "v_mfma_f32_16x16x32_bf16 v[228:231], a[48:51], v[48:51], v[228:231]\n\t"
    "v_mul_f32 v207, v207, v245\n\t"
    "v_cvt_pk_bf16_f32 v203, v206, v207\n\t"
    "v_mfma_f32_16x16x32_bf16 v[96:99], v[176:179], v[192:195], v[96:99]\n\t"
    "ds_read_b128 v[44:47], v14 offset:17600\n\t"
    "v_mov_b32_dpp v182, v164 quad_perm:[0,1,2,3] row_mask:0x3 bank_mask:0xf\n\t"
    "v_mfma_f32_16x16x32_bf16 v[64:67], v[176:179], v[196:199], v[64:67]\n\t"
    "ds_read_b128 v[40:43], v14 offset:17536\n\t"
    "v_mov_b32_dpp v183, v165 quad_perm:[0,1,2,3] row_mask:0x3 bank_mask:0xf\n\t"
    "v_mfma_f32_16x16x32_bf16 v[80:83], v[176:179], v[200:203], v[80:83]\n\t"
    "ds_read_b128 v[36:39], v14 offset:17472\n\t"
    "v_mov_b32_dpp v186, v168 quad_perm:[0,1,2,3] row_mask:0x3 bank_mask:0xf\n\t"
    "v_mfma_f32_16x16x32_bf16 v[84:87], v[180:183], v[200:203], v[84:87]\n\t"
    "ds_read_b128 v[32:35], v14 offset:17408\n\t"
    "v_mov_b32_dpp v187, v169 quad_perm:[0,1,2,3] row_mask:0x3 bank_mask:0xf\n\t"
    "v_mfma_f32_16x16x32_bf16 v[68:71], v[180:183], v[196:199], v[68:71]\n\t"
    "v_cvt_pk_bf16_f32 v172, v232, v233\n\t"
    "v_cvt_pk_bf16_f32 v173, v234, v235\n\t"
    "v_mfma_f32_16x16x32_bf16 v[100:103], v[180:183], v[192:195], v[100:103]\n\t"
    "v_mov_b32_dpp v190, v172 quad_perm:[0,1,2,3] row_mask:0x3 bank_mask:0xf\n\t"
    "v_mov_b32_dpp v191, v173 quad_perm:[0,1,2,3] row_mask:0x3 bank_mask:0xf\n\t"
    "v_mfma_f32_16x16x32_bf16 v[104:107], v[184:187], v[192:195], v[104:107]\n\t"
    "v_cvt_pk_bf16_f32 v188, v236, v237\n\t"
    "v_cvt_pk_bf16_f32 v189, v238, v239\n\t"
    "v_mfma_f32_16x16x32_bf16 v[72:75], v[184:187], v[196:199], v[72:75]\n\t"
    "s_waitcnt lgkmcnt(0)\n\t"
    "v_cvt_pk_bf16_f32 v120, v216, v217\n\t"
    "v_mfma_f32_16x16x32_bf16 v[88:91], v[184:187], v[200:203], v[88:91]\n\t"
    "v_cvt_pk_bf16_f32 v121, v218, v219\n\t"
    "v_cvt_pk_bf16_f32 v122, v220, v221\n\t"
    "v_mfma_f32_16x16x32_bf16 v[92:95], v[188:191], v[200:203], v[92:95]\n\t"
    "v_cvt_pk_bf16_f32 v123, v222, v223\n\t"
    "v_cvt_pk_bf16_f32 v124, v224, v225\n\t"
    "v_mfma_f32_16x16x32_bf16 v[76:79], v[188:191], v[196:199], v[76:79]\n\t"
    "v_cvt_pk_bf16_f32 v125, v226, v227\n\t"
    "v_cvt_pk_bf16_f32 v126, v228, v229\n\t"
    "v_mfma_f32_16x16x32_bf16 v[108:111], v[188:191], v[192:195], v[108:111]\n\t"
    "v_cvt_pk_bf16_f32 v127, v230, v231\n\t"
    "v_cvt_pk_bf16_f32 v80, v80, v81\n\t"
    "v_mfma_f32_16x16x32_bf16 v[232:235], a[0:3], v[32:35], 0\n\t"
    "v_cvt_pk_bf16_f32 v88, v88, v89\n\t"
    "v_cvt_pk_bf16_f32 v81, v82, v83\n\t"
    "v_mfma_f32_16x16x32_bf16 v[232:235], a[4:7], v[36:39], v[232:235]\n\t"
    "v_cvt_pk_bf16_f32 v89, v90, v91\n\t"
    "v_cvt_pk_bf16_f32 v82, v84, v85\n\t"
    "v_mfma_f32_16x16x32_bf16 v[232:235], a[8:11], v[40:43], v[232:235]\n\t"
    "v_cvt_pk_bf16_f32 v90, v92, v93\n\t"
    "v_cvt_pk_bf16_f32 v83, v86, v87\n\t"
    "v_mfma_f32_16x16x32_bf16 v[232:235], a[12:15], v[44:47], v[232:235]\n\t"
    "v_cvt_pk_bf16_f32 v91, v94, v95\n\t"
    "v_pk_max_i16 v80, v80, 0\n\t"
    "v_mfma_f32_16x16x32_bf16 v[236:239], a[28:31], v[44:47], 0\n\t"
    "v_pk_max_i16 v81, v81, 0\n\t"
    "v_pk_max_i16 v82, v82, 0\n\t"
    "v_mfma_f32_16x16x32_bf16 v[236:239], a[24:27], v[40:43], v[236:239]\n\t"
    "v_pk_max_i16 v83, v83, 0\n\t"
    "v_pk_max_i16 v88, v88, 0\n\t"
    "v_mfma_f32_16x16x32_bf16 v[236:239], a[20:23], v[36:39], v[236:239]\n\t"
    "v_pk_max_i16 v89, v89, 0\n\t"
    "v_pk_max_i16 v90, v90, 0\n\t"
    "v_mfma_f32_16x16x32_bf16 v[236:239], a[16:19], v[32:35], v[236:239]\n\t"
    "v_cvt_pk_bf16_f32 v128, v232, v233\n\t"
    "v_cvt_pk_bf16_f32 v129, v234, v235\n\t"
    "v_mfma_f32_16x16x32_bf16 v[216:219], a[32:35], v[32:35], 0\n\t"
    "v_pk_max_i16 v91, v91, 0\n\t"
    "v_cvt_pk_bf16_f32 v96, v96, v97\n\t"
    "v_mfma_f32_16x16x32_bf16 v[216:219], a[36:39], v[36:39], v[216:219]\n\t"
    "v_cvt_pk_bf16_f32 v104, v104, v105\n\t"
    "v_cvt_pk_bf16_f32 v97, v98, v99\n\t"
    "v_mfma_f32_16x16x32_bf16 v[216:219], a[40:43], v[40:43], v[216:219]\n\t"
    "v_cvt_pk_bf16_f32 v105, v106, v107\n\t"
    "v_cndmask_b32_dpp v92, v88, v80, vcc row_ror:8 row_mask:0xf bank_mask:0xf\n\t"
    "v_mfma_f32_16x16x32_bf16 v[216:219], a[44:47], v[44:47], v[216:219]\n\t"
    "v_cndmask_b32_dpp v93, v89, v81, vcc row_ror:8 row_mask:0xf bank_mask:0xf\n\t"
    "v_cndmask_b32_dpp v94, v90, v82, vcc row_ror:8 row_mask:0xf bank_mask:0xf\n\t"
    "v_mfma_f32_16x16x32_bf16 v[220:223], a[60:63], v[44:47], 0\n\t"
    "s_add_u32 m0, s33, 17408\n\t"
    "v_cndmask_b32_dpp v95, v91, v83, vcc row_ror:8 row_mask:0xf bank_mask:0xf\n\t"
    "v_mfma_f32_16x16x32_bf16 v[220:223], a[56:59], v[40:43], v[220:223]\n\t"
    "buffer_load_dwordx4 v248, s[20:23], s72 offen offset:0 nt lds\n\t"
    "v_cvt_pk_bf16_f32 v98, v100, v101\n\t"
    "v_mfma_f32_16x16x32_bf16 v[220:223], a[52:55], v[36:39], v[220:223]\n\t"
    "v_cvt_pk_bf16_f32 v106, v108, v109\n\t"
    "v_mov_b32_dpp v88, v80 row_ror:8 row_mask:0xf bank_mask:0x3\n\t"
    "v_mfma_f32_16x16x32_bf16 v[220:223], a[48:51], v[32:35], v[220:223]\n\t"
    "v_mov_b32_dpp v89, v81 row_ror:8 row_mask:0xf bank_mask:0x3\n\t"
    "v_mov_b32_dpp v90, v82 row_ror:8 row_mask:0xf bank_mask:0x3\n\t"
    "v_mfma_f32_16x16x32_bf16 v[224:227], a[64:67], v[32:35], 0\n\t"
    "buffer_load_dwordx4 v249, s[20:23], s72 offen offset:1088 nt lds\n\t"
    "v_mov_b32_dpp v91, v83 row_ror:8 row_mask:0xf bank_mask:0x3\n\t"
    "v_mfma_f32_16x16x32_bf16 v[224:227], a[68:71], v[36:39], v[224:227]\n\t"
    "v_cvt_pk_bf16_f32 v99, v102, v103\n\t"
    "v_cvt_pk_bf16_f32 v107, v110, v111\n\t"
    "v_mfma_f32_16x16x32_bf16 v[224:227], a[72:75], v[40:43], v[224:227]\n\t"
    "v_cvt_pk_bf16_f32 v130, v236, v237\n\t"
    "v_cvt_pk_bf16_f32 v131, v238, v239\n\t"
    "v_mfma_f32_16x16x32_bf16 v[224:227], a[76:79], v[44:47], v[224:227]\n\t"
    "buffer_load_dwordx4 v250, s[20:23], s72 offen offset:2176 nt lds\n\t"
    "v_pk_max_i16 v96, v96, 0\n\t"
    "v_mfma_f32_16x16x32_bf16 v[228:231], a[76:79], v[60:63], 0\n\t"
    "v_pk_max_i16 v97, v97, 0\n\t"
    "v_pk_max_i16 v98, v98, 0\n\t"
    "v_mfma_f32_16x16x32_bf16 v[228:231], a[72:75], v[56:59], v[228:231]\n\t"
    "v_pk_max_i16 v99, v99, 0\n\t"
    "v_pk_max_i16 v104, v104, 0\n\t"
    "v_mfma_f32_16x16x32_bf16 v[228:231], a[68:71], v[52:55], v[228:231]\n\t"
    "v_pk_max_i16 v105, v105, 0\n\t"
    "v_pk_max_i16 v106, v106, 0\n\t"
    "v_mfma_f32_16x16x32_bf16 v[228:231], a[64:67], v[48:51], v[228:231]\n\t"
    "buffer_load_dwordx4 v251, s[20:23], s72 offen offset:3264 nt lds\n\t"
    "v_pk_max_i16 v107, v107, 0\n\t"
    "v_mfma_f32_16x16x32_bf16 v[232:235], a[80:83], v[48:51], 0\n\t"
    "buffer_store_dwordx4 v[88:91], v12, s[24:27], s45 offen offset:2048" STPOL "\n\t"
    "v_mfma_f32_16x16x32_bf16 v[232:235], a[84:87], v[52:55], v[232:235]\n\t"
    "v_cndmask_b32_dpp v108, v104, v96, vcc row_ror:8 row_mask:0xf bank_mask:0xf\n\t"
    "v_cndmask_b32_dpp v109, v105, v97, vcc row_ror:8 row_mask:0xf bank_mask:0xf\n\t"
    "v_mfma_f32_16x16x32_bf16 v[232:235], a[88:91], v[56:59], v[232:235]\n\t"
    "v_cndmask_b32_dpp v110, v106, v98, vcc row_ror:8 row_mask:0xf bank_mask:0xf\n\t"
    "v_cndmask_b32_dpp v111, v107, v99, vcc row_ror:8 row_mask:0xf bank_mask:0xf\n\t"
    "v_mfma_f32_16x16x32_bf16 v[232:235], a[92:95], v[60:63], v[232:235]\n\t"
    "v_mov_b32_dpp v104, v96 row_ror:8 row_mask:0xf bank_mask:0x3\n\t"
    "v_mov_b32_dpp v105, v97 row_ror:8 row_mask:0xf bank_mask:0x3\n\t"
    "v_mfma_f32_16x16x32_bf16 v[236:239], a[92:95], v[44:47], 0\n\t"
    "v_mov_b32_dpp v106, v98 row_ror:8 row_mask:0xf bank_mask:0x3\n\t"
    "v_mov_b32_dpp v107, v99 row_ror:8 row_mask:0xf bank_mask:0x3\n\t"
    "v_mfma_f32_16x16x32_bf16 v[236:239], a[88:91], v[40:43], v[236:239]\n\t"
    "v_cvt_pk_bf16_f32 v132, v216, v217\n\t"
    "v_cvt_pk_bf16_f32 v133, v218, v219\n\t"
    "v_mfma_f32_16x16x32_bf16 v[236:239], a[84:87], v[36:39], v[236:239]\n\t"
    "v_cvt_pk_bf16_f32 v134, v220, v221\n\t"
    "v_cvt_pk_bf16_f32 v135, v222, v223\n\t"
    "v_mfma_f32_16x16x32_bf16 v[236:239], a[80:83], v[32:35], v[236:239]\n\t"
    "v_cvt_pk_bf16_f32 v152, v224, v225\n\t"
    "v_cvt_pk_bf16_f32 v153, v226, v227\n\t"
    "v_mfma_f32_16x16x32_bf16 v[216:219], a[96:99], v[32:35], 0\n\t"
    "v_cvt_pk_bf16_f32 v144, v228, v229\n\t"
    "v_cvt_pk_bf16_f32 v145, v230, v231\n\t"
    "v_mfma_f32_16x16x32_bf16 v[216:219], a[100:103], v[36:39], v[216:219]\n\t"
    "v_cvt_pk_bf16_f32 v146, v232, v233\n\t"
    "v_cvt_pk_bf16_f32 v147, v234, v235\n\t"
    "v_mfma_f32_16x16x32_bf16 v[216:219], a[104:107], v[40:43], v[216:219]\n\t"
    "v_mov_b32_dpp v112, v128 quad_perm:[0,1,2,3] row_mask:0xf bank_mask:0x3\n\t"
    "v_mov_b32_dpp v113, v129 quad_perm:[0,1,2,3] row_mask:0xf bank_mask:0x3\n\t"
    "v_mfma_f32_16x16x32_bf16 v[216:219], a[108:111], v[44:47], v[216:219]\n\t"
    "v_cvt_pk_bf16_f32 v154, v236, v237\n\t"
    "v_cvt_pk_bf16_f32 v155, v238, v239\n\t"
    "v_mfma_f32_16x16x32_bf16 v[220:223], a[108:111], v[60:63], 0\n\t"
    "buffer_store_dwordx4 v[92:95], v12, s[24:27], s45 offen offset:0" STPOL "\n\t"
    "v_mfma_f32_16x16x32_bf16 v[220:223], a[104:107], v[56:59], v[220:223]\n\t"
    "v_mov_b32_dpp v114, v130 quad_perm:[0,1,2,3] row_mask:0xf bank_mask:0x3\n\t"
    "v_mov_b32_dpp v115, v131 quad_perm:[0,1,2,3] row_mask:0xf bank_mask:0x3\n\t"
    "v_mfma_f32_16x16x32_bf16 v[220:223], a[100:103], v[52:55], v[220:223]\n\t"
    "v_mov_b32_dpp v136, v152 quad_perm:[0,1,2,3] row_mask:0xf bank_mask:0x3\n\t"
    "v_mov_b32_dpp v137, v153 quad_perm:[0,1,2,3] row_mask:0xf bank_mask:0x3\n\t"
    "v_mfma_f32_16x16x32_bf16 v[220:223], a[96:99], v[48:51], v[220:223]\n\t"
    "v_mov_b32_dpp v138, v154 quad_perm:[0,1,2,3] row_mask:0xf bank_mask:0x3\n\t"
    "v_mov_b32_dpp v139, v155 quad_perm:[0,1,2,3] row_mask:0xf bank_mask:0x3\n\t"
    "v_mfma_f32_16x16x32_bf16 v[224:227], a[112:115], v[48:51], 0\n\t"
    "v_cvt_pk_bf16_f32 v156, v216, v217\n\t"
    "v_cvt_pk_bf16_f32 v157, v218, v219\n\t"
    "v_mfma_f32_16x16x32_bf16 v[224:227], a[116:119], v[52:55], v[224:227]\n\t"
    "v_mov_b32_dpp v116, v132 quad_perm:[0,1,2,3] row_mask:0xf bank_mask:0x3\n\t"
    "v_mov_b32_dpp v117, v133 quad_perm:[0,1,2,3] row_mask:0xf bank_mask:0x3\n\t"
    "v_mfma_f32_16x16x32_bf16 v[224:227], a[120:123], v[56:59], v[224:227]\n\t"
    "v_mov_b32_dpp v118, v134 quad_perm:[0,1,2,3] row_mask:0xf bank_mask:0x3\n\t"
    "v_mov_b32_dpp v119, v135 quad_perm:[0,1,2,3] row_mask:0xf bank_mask:0x3\n\t"
    "v_mfma_f32_16x16x32_bf16 v[224:227], a[124:127], v[60:63], v[224:227]\n\t"
    "v_mov_b32_dpp v140, v156 quad_perm:[0,1,2,3] row_mask:0xf bank_mask:0x3\n\t"
    "v_mov_b32_dpp v141, v157 quad_perm:[0,1,2,3] row_mask:0xf bank_mask:0x3\n\t"
    "v_mfma_f32_16x16x32_bf16 v[228:231], a[124:127], v[44:47], 0\n\t"
    "v_cvt_pk_bf16_f32 v148, v220, v221\n\t"
    "v_cvt_pk_bf16_f32 v149, v222, v223\n\t"
    "v_mfma_f32_16x16x32_bf16 v[228:231], a[120:123], v[40:43], v[228:231]\n\t"
    "v_mfma_f32_16x16x32_bf16 v[228:231], a[116:119], v[36:39], v[228:231]\n\t"
    "v_mfma_f32_16x16x32_bf16 v[228:231], a[112:115], v[32:35], v[228:231]\n\t"
    "v_cvt_pk_bf16_f32 v150, v224, v225\n\t"
    "v_cvt_pk_bf16_f32 v151, v226, v227\n\t"
    "v_mfma_f32_16x16x32_bf16 v[232:235], v[48:51], a[128:131], 0\n\t"
    "buffer_store_dwordx4 v[108:111], v12, s[24:27], s43 offen offset:0" STPOL "\n\t"
    "v_mfma_f32_16x16x32_bf16 v[232:235], v[52:55], a[132:135], v[232:235]\n\t"
    "v_mfma_f32_16x16x32_bf16 v[232:235], v[56:59], a[136:139], v[232:235]\n\t"
    "v_mfma_f32_16x16x32_bf16 v[232:235], v[60:63], a[140:143], v[232:235]\n\t"
    "v_cvt_pk_bf16_f32 v158, v228, v229\n\t"
    "v_cvt_pk_bf16_f32 v159, v230, v231\n\t"
    "v_mfma_f32_16x16x32_bf16 v[236:239], v[44:47], a[140:143], 0\n\t"
    "v_mov_b32_dpp v142, v158 quad_perm:[0,1,2,3] row_mask:0xf bank_mask:0x3\n\t"
    "v_mov_b32_dpp v143, v159 quad_perm:[0,1,2,3] row_mask:0xf bank_mask:0x3\n\t"
    "v_mfma_f32_16x16x32_bf16 v[236:239], v[40:43], a[136:139], v[236:239]\n\t"
    "v_mfma_f32_16x16x32_bf16 v[236:239], v[36:39], a[132:135], v[236:239]\n\t"
    "v_mfma_f32_16x16x32_bf16 v[236:239], v[32:35], a[128:131], v[236:239]\n\t"
    "v_cvt_pk_bf16_f32 v162, v232, v233\n\t"
    "v_cvt_pk_bf16_f32 v163, v234, v235\n\t"
    "v_mfma_f32_16x16x32_bf16 v[192:195], v[136:139], v[112:115], v[208:211]\n\t"
    "v_mfma_f32_16x16x32_bf16 v[200:203], v[136:139], v[120:123], v[208:211]\n\t"
    "v_mfma_f32_16x16x32_bf16 v[204:207], v[144:147], v[120:123], 0\n\t"
    "v_mfma_f32_16x16x32_bf16 v[196:199], v[144:147], v[112:115], 0\n\t"
    "v_cvt_pk_bf16_f32 v176, v236, v237\n\t"
    "v_cvt_pk_bf16_f32 v177, v238, v239\n\t"
    "v_mfma_f32_16x16x32_bf16 v[192:195], v[140:143], v[116:119], v[192:195]\n\t"
    "buffer_store_dwordx4 v[104:107], v17, s[24:27], s43 offen offset:2048" STPOL "\n\t"
    "v_mfma_f32_16x16x32_bf16 v[200:203], v[140:143], v[124:127], v[200:203]\n\t"
    "v_mov_b32_dpp v160, v176 quad_perm:[0,1,2,3] row_mask:0x3 bank_mask:0xf\n\t"
    "v_mov_b32_dpp v161, v177 quad_perm:[0,1,2,3] row_mask:0x3 bank_mask:0xf\n\t"
    "v_mfma_f32_16x16x32_bf16 v[204:207], v[148:151], v[124:127], v[204:207]\n\t"
    "v_mfma_f32_16x16x32_bf16 v[196:199], v[148:151], v[116:119], v[196:199]\n\t"
    "v_mfma_f32_16x16x32_bf16 v[216:219], v[32:35], a[144:147], 0\n\t"
    "ds_read_b128 v[208:211], v13 offset:384\n\t"
    "ds_read_b128 v[212:215], v13 offset:448\n\t"
    "v_mfma_f32_16x16x32_bf16 v[216:219], v[36:39], a[148:151], v[216:219]\n\t"
    "v_max3_f32 v240, v192, v193, v194\n\t"
    "v_max3_f32 v241, v200, v201, v202\n\t"
    "v_mfma_f32_16x16x32_bf16 v[216:219], v[40:43], a[152:155], v[216:219]\n\t"
    "v_max3_f32 v243, v204, v205, v206\n\t"
    "v_max3_f32 v241, v241, v203, v207\n\t"
    "v_mfma_f32_16x16x32_bf16 v[216:219], v[44:47], a[156:159], v[216:219]\n\t"
    "v_max3_f32 v242, v196, v197, v198\n\t"
    "v_max3_f32 v240, v240, v195, v199\n\t"
    "v_mfma_f32_16x16x32_bf16 v[220:223], v[60:63], a[156:159], 0\n\t"
    "v_max_f32 v240, v240, v242\n\t"
    "v_max_f32 v241, v241, v243\n\t"
    "v_mfma_f32_16x16x32_bf16 v[220:223], v[56:59], a[152:155], v[220:223]\n\t"
    "s_nop 0\n\t"
    "v_permlane32_swap_b32 v240, v241\n\t"
    "v_mfma_f32_16x16x32_bf16 v[220:223], v[52:55], a[148:151], v[220:223]\n\t"
    "v_max_f32 v240, v240, v241\n\t"
    "v_mov_b32 v242, v240\n\t"
    "v_mfma_f32_16x16x32_bf16 v[220:223], v[48:51], a[144:147], v[220:223]\n\t"
    "s_nop 0\n\t"
    "v_permlane16_swap_b32 v240, v242\n\t"
    "v_mfma_f32_16x16x32_bf16 v[224:227], v[48:51], a[160:163], 0\n\t"
    "v_max_f32 v240, v240, v242\n\t"
    "v_mul_f32 v241, 0xbe38aa3b, v240\n\t"
    "v_mfma_f32_16x16x32_bf16 v[224:227], v[52:55], a[164:167], v[224:227]\n\t"
    "v_mul_f32 v240, 0xbe38aa3b, v240\n\t"
    "v_cvt_pk_bf16_f32 v180, v216, v217\n\t"
    "v_mfma_f32_16x16x32_bf16 v[224:227], v[56:59], a[168:171], v[224:227]\n\t"
    "v_permlane32_swap_b32 v240, v241\n\t"
    "v_mfma_f32_16x16x32_bf16 v[224:227], v[60:63], a[172:175], v[224:227]\n\t"
    "v_fmamk_f32 v192, v192, 0x3e38aa3b, v240\n\t"
    "v_fmamk_f32 v193, v193, 0x3e38aa3b, v240\n\t"
    "v_mfma_f32_16x16x32_bf16 v[228:231], v[44:47], a[172:175], 0\n\t"
    "v_fmamk_f32 v194, v194, 0x3e38aa3b, v240\n\t"
    "v_fmamk_f32 v195, v195, 0x3e38aa3b, v240\n\t"
    "v_mfma_f32_16x16x32_bf16 v[228:231], v[40:43], a[168:171], v[228:231]\n\t"
    "v_fmamk_f32 v200, v200, 0x3e38aa3b, v241\n\t"
    "v_fmamk_f32 v201, v201, 0x3e38aa3b, v241\n\t"
    "v_mfma_f32_16x16x32_bf16 v[228:231], v[36:39], a[164:167], v[228:231]\n\t"
    "v_fmamk_f32 v202, v202, 0x3e38aa3b, v241\n\t"
    "v_fmamk_f32 v203, v203, 0x3e38aa3b, v241\n\t"
    "v_mfma_f32_16x16x32_bf16 v[228:231], v[32:35], a[160:163], v[228:231]\n\t"
    "v_exp_f32 v192, v192\n\t"
    "v_mfma_f32_16x16x32_bf16 v[232:235], v[32:35], a[176:179], 0\n\t"
    "v_exp_f32 v193, v193\n\t"
    "v_mfma_f32_16x16x32_bf16 v[232:235], v[36:39], a[180:183], v[232:235]\n\t"
    "v_exp_f32 v194, v194\n\t"
    "v_mfma_f32_16x16x32_bf16 v[232:235], v[40:43], a[184:187], v[232:235]\n\t"
    "v_exp_f32 v195, v195\n\t"
    "v_mfma_f32_16x16x32_bf16 v[232:235], v[44:47], a[188:191], v[232:235]\n\t"
    "v_exp_f32 v200, v200\n\t"
    "v_mfma_f32_16x16x32_bf16 v[236:239], v[60:63], a[188:191], 0\n\t"
    "v_exp_f32 v201, v201\n\t"
    "v_mfma_f32_16x16x32_bf16 v[236:239], v[56:59], a[184:187], v[236:239]\n\t"
    "v_exp_f32 v202, v202\n\t"
    "v_mfma_f32_16x16x32_bf16 v[236:239], v[52:55], a[180:183], v[236:239]\n\t"
    "v_exp_f32 v203, v203\n\t"
    "v_mfma_f32_16x16x32_bf16 v[236:239], v[48:51], a[176:179], v[236:239]\n\t"
    "v_fmamk_f32 v196, v196, 0x3e38aa3b, v240\n\t"
    "v_fmamk_f32 v197, v197, 0x3e38aa3b, v240\n\t"
    "v_mfma_f32_16x16x32_bf16 v[80:83], a[192:195], v[48:51], 0\n\t"
    "v_fmamk_f32 v204, v204, 0x3e38aa3b, v241\n\t"
    "v_fmamk_f32 v205, v205, 0x3e38aa3b, v241\n\t"
    "v_mfma_f32_16x16x32_bf16 v[80:83], a[196:199], v[52:55], v[80:83]\n\t"
    "v_cvt_pk_bf16_f32 v181, v218, v219\n\t"
    "v_fmamk_f32 v198, v198, 0x3e38aa3b, v240\n\t"
    "v_mfma_f32_16x16x32_bf16 v[80:83], a[200:203], v[56:59], v[80:83]\n\t"
    "v_exp_f32 v196, v196\n\t"
    "v_mfma_f32_16x16x32_bf16 v[80:83], a[204:207], v[60:63], v[80:83]\n\t"
    "v_exp_f32 v197, v197\n\t"
    "v_mfma_f32_16x16x32_bf16 v[84:87], a[220:223], v[60:63], 0\n\t"
    "v_exp_f32 v204, v204\n\t"
    "v_mfma_f32_16x16x32_bf16 v[84:87], a[216:219], v[56:59], v[84:87]\n\t"
    "v_exp_f32 v205, v205\n\t"
    "v_mfma_f32_16x16x32_bf16 v[84:87], a[212:215], v[52:55], v[84:87]\n\t"
    "v_fmamk_f32 v199, v199, 0x3e38aa3b, v240\n\t"
    "v_fmamk_f32 v206, v206, 0x3e38aa3b, v241\n\t"
    "v_mfma_f32_16x16x32_bf16 v[84:87], a[208:211], v[48:51], v[84:87]\n\t"
    "v_fmamk_f32 v207, v207, 0x3e38aa3b, v241\n\t"
    "v_add_f32 v244, v192, v193\n\t"
    "v_mfma_f32_16x16x32_bf16 v[88:91], a[224:227], v[48:51], 0\n\t"
    "v_add_f32 v246, v194, v195\n\t"
    "v_add_f32 v245, v200, v201\n\t"
    "v_mfma_f32_16x16x32_bf16 v[88:91], a[228:231], v[52:55], v[88:91]\n\t"
    "v_add_f32 v247, v202, v203\n\t"
    "v_add_f32 v244, v244, v196\n\t"
    "v_mfma_f32_16x16x32_bf16 v[88:91], a[232:235], v[56:59], v[88:91]\n\t"
    "v_exp_f32 v198, v198\n\t"
    "v_mfma_f32_16x16x32_bf16 v[88:91], a[236:239], v[60:63], v[88:91]\n\t"
    "s_waitcnt vmcnt(20)\n\t"
    "v_add_f32 v246, v246, v197\n\t"
    "v_mfma_f32_16x16x32_bf16 v[92:95], a[252:255], v[60:63], 0\n\t"
    "ds_read_b128 v[60:63], v14 offset:35008\n\t"
    "v_add_f32 v245, v245, v204\n\t"
    "v_mfma_f32_16x16x32_bf16 v[92:95], a[248:251], v[56:59], v[92:95]\n\t"
    "ds_read_b128 v[56:59], v14 offset:34944\n\t"
    "v_add_f32 v247, v247, v205\n\t"
    "v_mfma_f32_16x16x32_bf16 v[92:95], a[244:247], v[52:55], v[92:95]\n\t"
    "ds_read_b128 v[52:55], v14 offset:34880\n\t"
    "v_add_f32 v244, v244, v198\n\t"
    "v_mfma_f32_16x16x32_bf16 v[92:95], a[240:243], v[48:51], v[92:95]\n\t"
    "ds_read_b128 v[48:51], v14 offset:34816\n\t"
    "v_cvt_pk_bf16_f32 v166, v220, v221\n\t"
    "v_mfma_f32_16x16x32_bf16 v[96:99], a[192:195], v[32:35], 0\n\t"
    ".Ls2_%=:\n\t"
    "s_cbranch_execz .Lret%=\n\t"
    "v_cvt_pk_bf16_f32 v167, v222, v223\n\t"
    "v_mfma_f32_16x16x32_bf16 v[96:99], a[196:199], v[36:39], v[96:99]\n\t"
    "v_exp_f32 v199, v199\n\t"
    "v_mfma_f32_16x16x32_bf16 v[96:99], a[200:203], v[40:43], v[96:99]\n\t"
    "v_exp_f32 v206, v206\n\t"
    "v_mfma_f32_16x16x32_bf16 v[96:99], a[204:207], v[44:47], v[96:99]\n\t"
    "v_exp_f32 v207, v207\n\t"
    "v_mfma_f32_16x16x32_bf16 v[100:103], a[220:223], v[44:47], 0\n\t"
    "v_add_f32 v246, v246, v199\n\t"
    "v_add_f32 v245, v245, v206\n\t"
    "v_mfma_f32_16x16x32_bf16 v[100:103], a[216:219], v[40:43], v[100:103]\n\t"
    "v_add_f32 v247, v247, v207\n\t"
    "v_add_f32 v244, v244, v246\n\t"
    "v_mfma_f32_16x16x32_bf16 v[100:103], a[212:215], v[36:39], v[100:103]\n\t"
    "v_add_f32 v245, v245, v247\n\t"
    "v_cvt_pk_bf16_f32 v170, v224, v225\n\t"
    "v_mfma_f32_16x16x32_bf16 v[100:103], a[208:211], v[32:35], v[100:103]\n\t"
    "s_waitcnt lgkmcnt(0)\n\t"
    "v_cvt_pk_bf16_f32 v171, v226, v227\n\t"
    "v_mfma_f32_16x16x32_bf16 v[104:107], a[224:227], v[32:35], 0\n\t"
    "v_permlane32_swap_b32 v244, v245\n\t"
    "v_mfma_f32_16x16x32_bf16 v[104:107], a[228:231], v[36:39], v[104:107]\n\t"
    "v_add_f32 v244, v244, v245\n\t"
    "v_mov_b32 v246, v244\n\t"
    "v_mfma_f32_16x16x32_bf16 v[104:107], a[232:235], v[40:43], v[104:107]\n\t"
    "s_nop 0\n\t"
    "v_permlane16_swap_b32 v244, v246\n\t"
    "v_mfma_f32_16x16x32_bf16 v[104:107], a[236:239], v[44:47], v[104:107]\n\t"
    "v_add_f32 v244, v244, v246\n\t"
    "v_cvt_pk_bf16_f32 v184, v228, v229\n\t"
    "v_mfma_f32_16x16x32_bf16 v[108:111], a[252:255], v[44:47], 0\n\t"
    "v_rcp_f32 v244, v244\n\t"
    "v_mfma_f32_16x16x32_bf16 v[108:111], a[248:251], v[40:43], v[108:111]\n\t"
    "v_mov_b32 v245, v244\n\t"
    "v_cvt_pk_bf16_f32 v185, v230, v231\n\t"
    "v_mfma_f32_16x16x32_bf16 v[108:111], a[244:247], v[36:39], v[108:111]\n\t"
    "v_permlane32_swap_b32 v244, v245\n\t"
    "v_mfma_f32_16x16x32_bf16 v[108:111], a[240:243], v[32:35], v[108:111]\n\t"
    "v_mul_f32 v192, v192, v244\n\t"
    "v_mul_f32 v193, v193, v244\n\t"
    "v_mfma_f32_16x16x32_bf16 v[216:219], a[0:3], v[48:51], 0\n\t"
    "v_cvt_pk_bf16_f32 v192, v192, v193\n\t"
    "v_mul_f32 v194, v194, v244\n\t"
    "v_mfma_f32_16x16x32_bf16 v[216:219], a[4:7], v[52:55], v[216:219]\n\t"
    "v_mul_f32 v195, v195, v244\n\t"
    "v_cvt_pk_bf16_f32 v193, v194, v195\n\t"
    "v_mfma_f32_16x16x32_bf16 v[216:219], a[8:11], v[56:59], v[216:219]\n\t"
    "v_mul_f32 v196, v196, v244\n\t"
    "v_mul_f32 v197, v197, v244\n\t"
    "v_mfma_f32_16x16x32_bf16 v[216:219], a[12:15], v[60:63], v[216:219]\n\t"
    "v_cvt_pk_bf16_f32 v194, v196, v197\n\t"
    "v_mul_f32 v198, v198, v244\n\t"
    "v_mfma_f32_16x16x32_bf16 v[220:223], a[28:31], v[60:63], 0\n\t"
    "v_mul_f32 v199, v199, v244\n\t"
    "v_cvt_pk_bf16_f32 v195, v198, v199\n\t"
    "v_mfma_f32_16x16x32_bf16 v[220:223], a[24:27], v[56:59], v[220:223]\n\t"
    "v_cndmask_b32 v196, 0, v192, s[50:51]\n\t"
    "v_cndmask_b32 v197, 0, v193, s[50:51]\n\t"
    "v_mfma_f32_16x16x32_bf16 v[220:223], a[20:23], v[52:55], v[220:223]\n\t"
    "v_cndmask_b32 v198, 0, v194, s[50:51]\n\t"
    "v_cndmask_b32 v199, 0, v195, s[50:51]\n\t"
    "v_mfma_f32_16x16x32_bf16 v[220:223], a[16:19], v[48:51], v[220:223]\n\t"
    "v_cndmask_b32 v192, v192, 0, s[56:57]\n\t"
    "v_cndmask_b32 v193, v193, 0, s[56:57]\n\t"
    "v_mfma_f32_16x16x32_bf16 v[224:227], a[32:35], v[48:51], 0\n\t"
    "v_cndmask_b32 v194, v194, 0, s[56:57]\n\t"
    "v_cndmask_b32 v195, v195, 0, s[56:57]\n\t"
    "v_mfma_f32_16x16x32_bf16 v[224:227], a[36:39], v[52:55], v[224:227]\n\t"
    "v_mul_f32 v200, v200, v245\n\t"
    "v_mul_f32 v201, v201, v245\n\t"
    "v_mfma_f32_16x16x32_bf16 v[224:227], a[40:43], v[56:59], v[224:227]\n\t"
    "v_cvt_pk_bf16_f32 v200, v200, v201\n\t"
    "v_mul_f32 v202, v202, v245\n\t"
    "v_mfma_f32_16x16x32_bf16 v[224:227], a[44:47], v[60:63], v[224:227]\n\t"
    "v_mul_f32 v203, v203, v245\n\t"
    "v_cvt_pk_bf16_f32 v201, v202, v203\n\t"
    "v_mfma_f32_16x16x32_bf16 v[228:231], a[60:63], v[60:63], 0\n\t"
    "v_mul_f32 v204, v204, v245\n\t"
    "v_mul_f32 v205, v205, v245\n\t"
    "v_mfma_f32_16x16x32_bf16 v[228:231], a[56:59], v[56:59], v[228:231]\n\t"
    "v_cvt_pk_bf16_f32 v202, v204, v205\n\t"
    "v_mul_f32 v206, v206, v245\n\t"
    "v_mfma_f32_16x16x32_bf16 v[228:231], a[52:55], v[52:55], v[228:231]\n\t"
    "v_mul_f32 v207, v207, v245\n\t"
    "v_cvt_pk_bf16_f32 v203, v206, v207\n\t"
    "v_mfma_f32_16x16x32_bf16 v[228:231], a[48:51], v[48:51], v[228:231]\n\t"
    "v_mov_b32_dpp v164, v180 quad_perm:[0,1,2,3] row_mask:0x3 bank_mask:0xf\n\t"
    "v_mov_b32_dpp v165, v181 quad_perm:[0,1,2,3] row_mask:0x3 bank_mask:0xf\n\t"
    "v_mfma_f32_16x16x32_bf16 v[64:67], v[160:163], v[192:195], v[64:67]\n\t"
    "v_mov_b32_dpp v168, v184 quad_perm:[0,1,2,3] row_mask:0x3 bank_mask:0xf\n\t"
    "v_mov_b32_dpp v169, v185 quad_perm:[0,1,2,3] row_mask:0x3 bank_mask:0xf\n\t"
    "v_mfma_f32_16x16x32_bf16 v[96:99], v[160:163], v[196:199], v[96:99]\n\t"
    "v_cvt_pk_bf16_f32 v188, v232, v233\n\t"
    "v_cvt_pk_bf16_f32 v189, v234, v235\n\t"
    "v_mfma_f32_16x16x32_bf16 v[80:83], v[160:163], v[200:203], v[80:83]\n\t"
    "v_mov_b32_dpp v172, v188 quad_perm:[0,1,2,3] row_mask:0x3 bank_mask:0xf\n\t"
    "v_mov_b32_dpp v173, v189 quad_perm:[0,1,2,3] row_mask:0x3 bank_mask:0xf\n\t"
    "v_mfma_f32_16x16x32_bf16 v[84:87], v[164:167], v[200:203], v[84:87]\n\t"
    "v_cvt_pk_bf16_f32 v174, v236, v237\n\t"
    "v_cvt_pk_bf16_f32 v175, v238, v239\n\t"
    "v_mfma_f32_16x16x32_bf16 v[100:103], v[164:167], v[196:199], v[100:103]\n\t"
    "v_cvt_pk_bf16_f32 v112, v216, v217\n\t"
    "v_cvt_pk_bf16_f32 v113, v218, v219\n\t"
    "v_mfma_f32_16x16x32_bf16 v[68:71], v[164:167], v[192:195], v[68:71]\n\t"
    "v_cvt_pk_bf16_f32 v114, v220, v221\n\t"
    "v_cvt_pk_bf16_f32 v115, v222, v223\n\t"
    "v_mfma_f32_16x16x32_bf16 v[72:75], v[168:171], v[192:195], v[72:75]\n\t"
    "v_cvt_pk_bf16_f32 v116, v224, v225\n\t"
    "v_cvt_pk_bf16_f32 v117, v226, v227\n\t"
    "v_mfma_f32_16x16x32_bf16 v[104:107], v[168:171], v[196:199], v[104:107]\n\t"
    "v_cvt_pk_bf16_f32 v118, v228, v229\n\t"
    "v_cvt_pk_bf16_f32 v119, v230, v231\n\t"
    "v_mfma_f32_16x16x32_bf16 v[88:91], v[168:171], v[200:203], v[88:91]\n\t"
    "v_cvt_pk_bf16_f32 v80, v80, v81\n\t"
    "v_cvt_pk_bf16_f32 v81, v82, v83\n\t"
    "v_mfma_f32_16x16x32_bf16 v[92:95], v[172:175], v[200:203], v[92:95]\n\t"
    "v_cvt_pk_bf16_f32 v82, v84, v85\n\t"
    "v_cvt_pk_bf16_f32 v83, v86, v87\n\t"
    "v_mfma_f32_16x16x32_bf16 v[108:111], v[172:175], v[196:199], v[108:111]\n\t"
    "v_pk_max_i16 v80, v80, 0\n\t"
    "v_pk_max_i16 v81, v81, 0\n\t"
    "v_mfma_f32_16x16x32_bf16 v[76:79], v[172:175], v[192:195], v[76:79]\n\t"
    "v_pk_max_i16 v82, v82, 0\n\t"
    "v_pk_max_i16 v83, v83, 0\n\t"
    "v_mfma_f32_16x16x32_bf16 v[232:235], a[64:67], v[48:51], 0\n\t"
    "v_cvt_pk_bf16_f32 v88, v88, v89\n\t"
    "v_cvt_pk_bf16_f32 v89, v90, v91\n\t"
    "v_mfma_f32_16x16x32_bf16 v[232:235], a[68:71], v[52:55], v[232:235]\n\t"
    "v_cvt_pk_bf16_f32 v90, v92, v93\n\t"
    "v_cvt_pk_bf16_f32 v91, v94, v95\n\t"
    "v_mfma_f32_16x16x32_bf16 v[232:235], a[72:75], v[56:59], v[232:235]\n\t"
    "v_pk_max_i16 v88, v88, 0\n\t"
    "v_pk_max_i16 v89, v89, 0\n\t"
    "v_mfma_f32_16x16x32_bf16 v[232:235], a[76:79], v[60:63], v[232:235]\n\t"
    "v_pk_max_i16 v90, v90, 0\n\t"
    "v_pk_max_i16 v91, v91, 0\n\t"
    "v_mfma_f32_16x16x32_bf16 v[236:239], a[92:95], v[60:63], 0\n\t"
    "v_cvt_pk_bf16_f32 v64, v64, v65\n\t"
    "v_cvt_pk_bf16_f32 v72, v72, v73\n\t"
    "v_mfma_f32_16x16x32_bf16 v[236:239], a[88:91], v[56:59], v[236:239]\n\t"
    "v_cvt_pk_bf16_f32 v65, v66, v67\n\t"
    "v_cvt_pk_bf16_f32 v73, v74, v75\n\t"
    "v_mfma_f32_16x16x32_bf16 v[236:239], a[84:87], v[52:55], v[236:239]\n\t"
    "v_cndmask_b32_dpp v92, v88, v80, vcc row_ror:8 row_mask:0xf bank_mask:0xf\n\t"
    "v_cndmask_b32_dpp v93, v89, v81, vcc row_ror:8 row_mask:0xf bank_mask:0xf\n\t"
    "v_mfma_f32_16x16x32_bf16 v[236:239], a[80:83], v[48:51], v[236:239]\n\t"
    "v_cvt_pk_bf16_f32 v136, v232, v233\n\t"
    "v_cvt_pk_bf16_f32 v137, v234, v235\n\t"
    "v_mfma_f32_16x16x32_bf16 v[216:219], a[96:99], v[48:51], 0\n\t"
    "v_cndmask_b32_dpp v94, v90, v82, vcc row_ror:8 row_mask:0xf bank_mask:0xf\n\t"
    "v_cndmask_b32_dpp v95, v91, v83, vcc row_ror:8 row_mask:0xf bank_mask:0xf\n\t"
    "v_mfma_f32_16x16x32_bf16 v[216:219], a[100:103], v[52:55], v[216:219]\n\t"
    "buffer_store_dwordx4 v[92:95], v12, s[24:27], s48 offen offset:0" STPOL "\n\t"
    "v_mfma_f32_16x16x32_bf16 v[216:219], a[104:107], v[56:59], v[216:219]\n\t"
    "v_cvt_pk_bf16_f32 v66, v68, v69\n\t"
    "v_cvt_pk_bf16_f32 v74, v76, v77\n\t"
    "v_mfma_f32_16x16x32_bf16 v[216:219], a[108:111], v[60:63], v[216:219]\n\t"
    "v_cvt_pk_bf16_f32 v138, v236, v237\n\t"
    "v_cvt_pk_bf16_f32 v139, v238, v239\n\t"
    "v_mfma_f32_16x16x32_bf16 v[220:223], a[124:127], v[60:63], 0\n\t"
    "v_mov_b32_dpp v88, v80 row_ror:8 row_mask:0xf bank_mask:0x3\n\t"
    "v_mov_b32_dpp v89, v81 row_ror:8 row_mask:0xf bank_mask:0x3\n\t"
    "v_mfma_f32_16x16x32_bf16 v[220:223], a[120:123], v[56:59], v[220:223]\n\t"
    "v_mov_b32_dpp v90, v82 row_ror:8 row_mask:0xf bank_mask:0x3\n\t"
    "v_mov_b32_dpp v91, v83 row_ror:8 row_mask:0xf bank_mask:0x3\n\t"
    "v_mfma_f32_16x16x32_bf16 v[220:223], a[116:119], v[52:55], v[220:223]\n\t"
    "v_cvt_pk_bf16_f32 v67, v70, v71\n\t"
    "v_cvt_pk_bf16_f32 v75, v78, v79\n\t"
    "v_mfma_f32_16x16x32_bf16 v[220:223], a[112:115], v[48:51], v[220:223]\n\t"
    "v_cvt_pk_bf16_f32 v140, v216, v217\n\t"
    "v_cvt_pk_bf16_f32 v141, v218, v219\n\t"
    "v_mfma_f32_16x16x32_bf16 v[224:227], v[48:51], a[128:131], 0\n\t"
    "v_pk_max_i16 v64, v64, 0\n\t"
    "v_pk_max_i16 v65, v65, 0\n\t"
    "v_mfma_f32_16x16x32_bf16 v[224:227], v[52:55], a[132:135], v[224:227]\n\t"
    "v_pk_max_i16 v66, v66, 0\n\t"
    "v_pk_max_i16 v67, v67, 0\n\t"
    "v_mfma_f32_16x16x32_bf16 v[224:227], v[56:59], a[136:139], v[224:227]\n\t"
    "v_pk_max_i16 v72, v72, 0\n\t"
    "v_pk_max_i16 v73, v73, 0\n\t"
    "v_mfma_f32_16x16x32_bf16 v[224:227], v[60:63], a[140:143], v[224:227]\n\t"
    "v_cvt_pk_bf16_f32 v142, v220, v221\n\t"
    "v_cvt_pk_bf16_f32 v143, v222, v223\n\t"
    "v_mfma_f32_16x16x32_bf16 v[228:231], v[60:63], a[156:159], 0\n\t"
    "v_pk_max_i16 v74, v74, 0\n\t"
    "v_pk_max_i16 v75, v75, 0\n\t"
    "v_mfma_f32_16x16x32_bf16 v[228:231], v[56:59], a[152:155], v[228:231]\n\t"
    "buffer_store_dwordx4 v[88:91], v12, s[24:27], s48 offen offset:2048" STPOL "\n\t"
    "v_mfma_f32_16x16x32_bf16 v[228:231], v[52:55], a[148:151], v[228:231]\n\t"
    "v_cndmask_b32_dpp v76, v72, v64, vcc row_ror:8 row_mask:0xf bank_mask:0xf\n\t"
    "v_cndmask_b32_dpp v77, v73, v65, vcc row_ror:8 row_mask:0xf bank_mask:0xf\n\t"
    "v_mfma_f32_16x16x32_bf16 v[228:231], v[48:51], a[144:147], v[228:231]\n\t"
    "v_cndmask_b32_dpp v78, v74, v66, vcc row_ror:8 row_mask:0xf bank_mask:0xf\n\t"
    "v_cndmask_b32_dpp v79, v75, v67, vcc row_ror:8 row_mask:0xf bank_mask:0xf\n\t"
    "v_mfma_f32_16x16x32_bf16 v[192:195], v[152:155], v[128:131], v[208:211]\n\t"
    "v_mov_b32_dpp v72, v64 row_ror:8 row_mask:0xf bank_mask:0x3\n\t"
    "v_mov_b32_dpp v73, v65 row_ror:8 row_mask:0xf bank_mask:0x3\n\t"
    "v_mfma_f32_16x16x32_bf16 v[200:203], v[152:155], v[112:115], v[208:211]\n\t"
    "v_mov_b32_dpp v74, v66 row_ror:8 row_mask:0xf bank_mask:0x3\n\t"
    "v_mov_b32_dpp v75, v67 row_ror:8 row_mask:0xf bank_mask:0x3\n\t"
    "v_mfma_f32_16x16x32_bf16 v[204:207], v[136:139], v[112:115], v[212:215]\n\t"
    "s_waitcnt vmcnt(16)\n\t"
    "v_cvt_pk_bf16_f32 v178, v224, v225\n\t"
    "v_mfma_f32_16x16x32_bf16 v[196:199], v[136:139], v[128:131], v[212:215]\n\t"
    "ds_read_b128 v[44:47], v11 offset:8896\n\t"
    "v_cvt_pk_bf16_f32 v179, v226, v227\n\t"
    "v_mfma_f32_16x16x32_bf16 v[192:195], v[156:159], v[132:135], v[192:195]\n\t"
    "ds_read_b128 v[40:43], v11 offset:8832\n\t"
    "v_cvt_pk_bf16_f32 v182, v228, v229\n\t"
    "v_mfma_f32_16x16x32_bf16 v[200:203], v[156:159], v[116:119], v[200:203]\n\t"
    "ds_read_b128 v[36:39], v11 offset:8768\n\t"
    "v_cvt_pk_bf16_f32 v183, v230, v231\n\t"
    "v_mfma_f32_16x16x32_bf16 v[204:207], v[140:143], v[116:119], v[204:207]\n\t"
    "ds_read_b128 v[32:35], v11 offset:8704\n\t"
    "v_mfma_f32_16x16x32_bf16 v[196:199], v[140:143], v[132:135], v[196:199]\n\t"
    "s_waitcnt lgkmcnt(0)\n\t"
    "v_mfma_f32_16x16x32_bf16 v[232:235], v[48:51], a[160:163], 0\n\t"
    "ds_read_b128 v[212:215], v13 offset:512\n\t"
    "v_max3_f32 v240, v192, v193, v194\n\t"
    "v_mfma_f32_16x16x32_bf16 v[232:235], v[52:55], a[164:167], v[232:235]\n\t"
    "buffer_store_dwordx4 v[76:79], v12, s[24:27], s46 offen offset:0" STPOL "\n\t"
    "v_mfma_f32_16x16x32_bf16 v[232:235], v[56:59], a[168:171], v[232:235]\n\t"
    "v_max3_f32 v241, v200, v201, v202\n\t"
    "v_max3_f32 v243, v204, v205, v206\n\t"
    "v_mfma_f32_16x16x32_bf16 v[232:235], v[60:63], a[172:175], v[232:235]\n\t"
    "v_max3_f32 v241, v241, v203, v207\n\t"
    "v_max3_f32 v242, v196, v197, v198\n\t"
    "v_mfma_f32_16x16x32_bf16 v[236:239], v[60:63], a[188:191], 0\n\t"
    "v_max3_f32 v240, v240, v195, v199\n\t"
    "v_max_f32 v240, v240, v242\n\t"
    "v_mfma_f32_16x16x32_bf16 v[236:239], v[56:59], a[184:187], v[236:239]\n\t"
    "v_max_f32 v241, v241, v243\n\t"
    "v_mfma_f32_16x16x32_bf16 v[236:239], v[52:55], a[180:183], v[236:239]\n\t"
    "s_nop 0\n\t"
    "v_permlane32_swap_b32 v240, v241\n\t"
    "v_mfma_f32_16x16x32_bf16 v[236:239], v[48:51], a[176:179], v[236:239]\n\t"
    "v_cvt_pk_bf16_f32 v186, v232, v233\n\t"
    "v_cvt_pk_bf16_f32 v187, v234, v235\n\t"
    "v_mfma_f32_16x16x32_bf16 v[80:83], a[192:195], v[48:51], 0\n\t"
    "v_max_f32 v240, v240, v241\n\t"
    "v_mov_b32 v242, v240\n\t"
    "v_mfma_f32_16x16x32_bf16 v[80:83], a[196:199], v[52:55], v[80:83]\n\t"
    "s_nop 0\n\t"
    "v_permlane16_swap_b32 v240, v242\n\t"
    "v_mfma_f32_16x16x32_bf16 v[80:83], a[200:203], v[56:59], v[80:83]\n\t"
    "v_max_f32 v240, v240, v242\n\t"
    "v_mul_f32 v241, 0xbe38aa3b, v240\n\t"
    "v_mfma_f32_16x16x32_bf16 v[80:83], a[204:207], v[60:63], v[80:83]\n\t"
    "v_cvt_pk_bf16_f32 v190, v236, v237\n\t"
    "v_cvt_pk_bf16_f32 v191, v238, v239\n\t"
    "v_mfma_f32_16x16x32_bf16 v[84:87], a[220:223], v[60:63], 0\n\t"
    "v_mul_f32 v240, 0xbe38aa3b, v240\n\t"
    "v_mfma_f32_16x16x32_bf16 v[84:87], a[216:219], v[56:59], v[84:87]\n\t"
    "buffer_store_dwordx4 v[72:75], v12, s[24:27], s47 offen offset:2048" STPOL "\n\t"
    "v_mfma_f32_16x16x32_bf16 v[84:87], a[212:215], v[52:55], v[84:87]\n\t"
    "v_permlane32_swap_b32 v240, v241\n\t"
    "v_mfma_f32_16x16x32_bf16 v[84:87], a[208:211], v[48:51], v[84:87]\n\t"
    "v_fmamk_f32 v192, v192, 0x3e38aa3b, v240\n\t"
    "v_fmamk_f32 v193, v193, 0x3e38aa3b, v240\n\t"
    "v_mfma_f32_16x16x32_bf16 v[88:91], a[224:227], v[48:51], 0\n\t"
    "v_fmamk_f32 v194, v194, 0x3e38aa3b, v240\n\t"
    "v_fmamk_f32 v195, v195, 0x3e38aa3b, v240\n\t"
    "v_mfma_f32_16x16x32_bf16 v[88:91], a[228:231], v[52:55], v[88:91]\n\t"
    "v_fmamk_f32 v200, v200, 0x3e38aa3b, v241\n\t"
    "v_fmamk_f32 v201, v201, 0x3e38aa3b, v241\n\t"
    "v_mfma_f32_16x16x32_bf16 v[88:91], a[232:235], v[56:59], v[88:91]\n\t"
    "v_fmamk_f32 v202, v202, 0x3e38aa3b, v241\n\t"
    "v_fmamk_f32 v203, v203, 0x3e38aa3b, v241\n\t"
    "v_mfma_f32_16x16x32_bf16 v[88:91], a[236:239], v[60:63], v[88:91]\n\t"
    "s_waitcnt vmcnt(14)\n\t"
    "v_fmamk_f32 v196, v196, 0x3e38aa3b, v240\n\t"
    "v_mfma_f32_16x16x32_bf16 v[92:95], a[252:255], v[60:63], 0\n\t"
    "ds_read_b128 v[60:63], v11 offset:13248\n\t"
    "v_fmamk_f32 v197, v197, 0x3e38aa3b, v240\n\t"
    "v_mfma_f32_16x16x32_bf16 v[92:95], a[248:251], v[56:59], v[92:95]\n\t"
    "ds_read_b128 v[56:59], v11 offset:13184\n\t"
    "v_fmamk_f32 v204, v204, 0x3e38aa3b, v241\n\t"
    "v_mfma_f32_16x16x32_bf16 v[92:95], a[244:247], v[52:55], v[92:95]\n\t"
    "ds_read_b128 v[52:55], v11 offset:13120\n\t"
    "v_fmamk_f32 v205, v205, 0x3e38aa3b, v241\n\t"
    "v_mfma_f32_16x16x32_bf16 v[92:95], a[240:243], v[48:51], v[92:95]\n\t"
    "ds_read_b128 v[48:51], v11 offset:13056\n\t"
    "v_fmamk_f32 v198, v198, 0x3e38aa3b, v240\n\t"
    "v_mfma_f32_16x16x32_bf16 v[216:219], a[0:3], v[32:35], 0\n\t"
    "v_exp_f32 v192, v192\n\t"
    "v_mfma_f32_16x16x32_bf16 v[216:219], a[4:7], v[36:39], v[216:219]\n\t"
    "v_exp_f32 v193, v193\n\t"
    "v_mfma_f32_16x16x32_bf16 v[216:219], a[8:11], v[40:43], v[216:219]\n\t"
    "v_exp_f32 v194, v194\n\t"
    "v_mfma_f32_16x16x32_bf16 v[216:219], a[12:15], v[44:47], v[216:219]\n\t"
    "v_exp_f32 v195, v195\n\t"
    "v_mfma_f32_16x16x32_bf16 v[220:223], a[28:31], v[44:47], 0\n\t"
    "v_exp_f32 v200, v200\n\t"
    "v_mfma_f32_16x16x32_bf16 v[220:223], a[24:27], v[40:43], v[220:223]\n\t"
    "v_exp_f32 v201, v201\n\t"
    "v_mfma_f32_16x16x32_bf16 v[220:223], a[20:23], v[36:39], v[220:223]\n\t"
    "v_exp_f32 v202, v202\n\t"
    "v_mfma_f32_16x16x32_bf16 v[220:223], a[16:19], v[32:35], v[220:223]\n\t"
    "s_waitcnt lgkmcnt(0)\n\t"
    "v_cvt_pk_bf16_f32 v120, v216, v217\n\t"
    "v_mfma_f32_16x16x32_bf16 v[224:227], a[32:35], v[32:35], 0\n\t"
    "v_cvt_pk_bf16_f32 v121, v218, v219\n\t"
    "v_fmamk_f32 v199, v199, 0x3e38aa3b, v240\n\t"
    "v_mfma_f32_16x16x32_bf16 v[224:227], a[36:39], v[36:39], v[224:227]\n\t"
    "v_exp_f32 v203, v203\n\t"
    "v_mfma_f32_16x16x32_bf16 v[224:227], a[40:43], v[40:43], v[224:227]\n\t"
    "v_exp_f32 v196, v196\n\t"
    "v_mfma_f32_16x16x32_bf16 v[224:227], a[44:47], v[44:47], v[224:227]\n\t"
    "v_cvt_pk_bf16_f32 v122, v220, v221\n\t"
    "v_cvt_pk_bf16_f32 v123, v222, v223\n\t"
    "v_mfma_f32_16x16x32_bf16 v[228:231], a[60:63], v[44:47], 0\n\t"
    "v_exp_f32 v197, v197\n\t"
    "v_mfma_f32_16x16x32_bf16 v[228:231], a[56:59], v[40:43], v[228:231]\n\t"
    "v_exp_f32 v204, v204\n\t"
    "v_mfma_f32_16x16x32_bf16 v[228:231], a[52:55], v[36:39], v[228:231]\n\t"
    "v_exp_f32 v205, v205\n\t"
    "v_mfma_f32_16x16x32_bf16 v[228:231], a[48:51], v[32:35], v[228:231]\n\t"
    "v_cvt_pk_bf16_f32 v124, v224, v225\n\t"
    "v_cvt_pk_bf16_f32 v125, v226, v227\n\t"
    "v_mfma_f32_16x16x32_bf16 v[232:235], a[0:3], v[48:51], 0\n\t"
    "v_fmamk_f32 v206, v206, 0x3e38aa3b, v241\n\t"
    "v_fmamk_f32 v207, v207, 0x3e38aa3b, v241\n\t"
    "v_mfma_f32_16x16x32_bf16 v[232:235], a[4:7], v[52:55], v[232:235]\n\t"
    "v_add_f32 v244, v192, v193\n\t"
    "v_add_f32 v246, v194, v195\n\t"
    "v_mfma_f32_16x16x32_bf16 v[232:235], a[8:11], v[56:59], v[232:235]\n\t"
    "v_add_f32 v245, v200, v201\n\t"
    "v_add_f32 v247, v202, v203\n\t"
    "v_mfma_f32_16x16x32_bf16 v[232:235], a[12:15], v[60:63], v[232:235]\n\t"
    "v_exp_f32 v198, v198\n\t"
    "v_mfma_f32_16x16x32_bf16 v[236:239], a[28:31], v[60:63], 0\n\t"
    "v_exp_f32 v199, v199\n\t"
    "v_mfma_f32_16x16x32_bf16 v[236:239], a[24:27], v[56:59], v[236:239]\n\t"
    "v_exp_f32 v206, v206\n\t"
    "v_mfma_f32_16x16x32_bf16 v[236:239], a[20:23], v[52:55], v[236:239]\n\t"
    "v_exp_f32 v207, v207\n\t"
    "v_mfma_f32_16x16x32_bf16 v[236:239], a[16:19], v[48:51], v[236:239]\n\t"
    "v_add_f32 v244, v244, v196\n\t"
    "v_add_f32 v246, v246, v197\n\t"
    "v_mfma_f32_16x16x32_bf16 v[216:219], a[32:35], v[48:51], 0\n\t"
    "v_add_f32 v245, v245, v204\n\t"
    "v_add_f32 v247, v247, v205\n\t"
    "v_mfma_f32_16x16x32_bf16 v[216:219], a[36:39], v[52:55], v[216:219]\n\t"
    "v_add_f32 v244, v244, v198\n\t"
    "v_add_f32 v246, v246, v199\n\t"
    "v_mfma_f32_16x16x32_bf16 v[216:219], a[40:43], v[56:59], v[216:219]\n\t"
    "v_add_f32 v245, v245, v206\n\t"
    "v_add_f32 v247, v247, v207\n\t"
    "v_mfma_f32_16x16x32_bf16 v[216:219], a[44:47], v[60:63], v[216:219]\n\t"
    "v_add_f32 v244, v244, v246\n\t"
    "v_add_f32 v245, v245, v247\n\t"
    "v_mfma_f32_16x16x32_bf16 v[220:223], a[60:63], v[60:63], 0\n\t"
    "v_cvt_pk_bf16_f32 v126, v228, v229\n\t"
    "v_cvt_pk_bf16_f32 v127, v230, v231\n\t"
    "v_mfma_f32_16x16x32_bf16 v[220:223], a[56:59], v[56:59], v[220:223]\n\t"
    "v_permlane32_swap_b32 v244, v245\n\t"
    "v_mfma_f32_16x16x32_bf16 v[220:223], a[52:55], v[52:55], v[220:223]\n\t"
    "v_add_f32 v244, v244, v245\n\t"
    "v_mov_b32 v246, v244\n\t"
    "v_mfma_f32_16x16x32_bf16 v[220:223], a[48:51], v[48:51], v[220:223]\n\t"
    "s_nop 0\n\t"
    "v_permlane16_swap_b32 v244, v246\n\t"
    "v_mfma_f32_16x16x32_bf16 v[224:227], a[64:67], v[48:51], 0\n\t"
    "v_cvt_pk_bf16_f32 v128, v232, v233\n\t"
    "v_cvt_pk_bf16_f32 v129, v234, v235\n\t"
    "v_mfma_f32_16x16x32_bf16 v[224:227], a[68:71], v[52:55], v[224:227]\n\t"
    "v_add_f32 v244, v244, v246\n\t"
    "v_cvt_pk_bf16_f32 v130, v236, v237\n\t"
    "v_mfma_f32_16x16x32_bf16 v[224:227], a[72:75], v[56:59], v[224:227]\n\t"
    "v_rcp_f32 v244, v244\n\t"
    "v_mfma_f32_16x16x32_bf16 v[224:227], a[76:79], v[60:63], v[224:227]\n\t"
    "v_mov_b32 v245, v244\n\t"
    "v_cvt_pk_bf16_f32 v131, v238, v239\n\t"
    "v_mfma_f32_16x16x32_bf16 v[228:231], a[76:79], v[44:47], 0\n\t"
    "v_permlane32_swap_b32 v244, v245\n\t"
    "v_mfma_f32_16x16x32_bf16 v[228:231], a[72:75], v[40:43], v[228:231]\n\t"
    "v_cndmask_b32 v244, v244, 0, s[50:51]\n\t"
    "v_mul_f32 v192, v192, v244\n\t"
    "v_mfma_f32_16x16x32_bf16 v[228:231], a[68:71], v[36:39], v[228:231]\n\t"
    "v_mul_f32 v193, v193, v244\n\t"
    "v_cvt_pk_bf16_f32 v192, v192, v193\n\t"
    "v_mfma_f32_16x16x32_bf16 v[228:231], a[64:67], v[32:35], v[228:231]\n\t"
    "v_mul_f32 v194, v194, v244\n\t"
    "v_mul_f32 v195, v195, v244\n\t"
    "v_mfma_f32_16x16x32_bf16 v[232:235], a[80:83], v[32:35], 0\n\t"
    "v_cndmask_b32 v245, 0, v245, s[60:61]\n\t"
    "v_cvt_pk_bf16_f32 v193, v194, v195\n\t"
    "v_mfma_f32_16x16x32_bf16 v[232:235], a[84:87], v[36:39], v[232:235]\n\t"
    "v_mul_f32 v196, v196, v244\n\t"
    "v_mul_f32 v197, v197, v244\n\t"
    "v_mfma_f32_16x16x32_bf16 v[232:235], a[88:91], v[40:43], v[232:235]\n\t"
    "v_mul_f32 v200, v200, v245\n\t"
    "v_mul_f32 v201, v201, v245\n\t"
    "v_mfma_f32_16x16x32_bf16 v[232:235], a[92:95], v[44:47], v[232:235]\n\t"
    "v_cvt_pk_bf16_f32 v194, v196, v197\n\t"
    "v_mul_f32 v198, v198, v244\n\t"
    "v_mfma_f32_16x16x32_bf16 v[236:239], a[92:95], v[60:63], 0\n\t"
    "v_mul_f32 v199, v199, v244\n\t"
    "v_cvt_pk_bf16_f32 v200, v200, v201\n\t"
    "v_mfma_f32_16x16x32_bf16 v[236:239], a[88:91], v[56:59], v[236:239]\n\t"
    "v_mul_f32 v202, v202, v245\n\t"
    "v_mul_f32 v203, v203, v245\n\t"
    "v_mfma_f32_16x16x32_bf16 v[236:239], a[84:87], v[52:55], v[236:239]\n\t"
    "v_cvt_pk_bf16_f32 v195, v198, v199\n\t"
    "v_cvt_pk_bf16_f32 v201, v202, v203\n\t"
    "v_mul_f32 v204, v204, v245\n\t"
    "v_mul_f32 v205, v205, v245\n\t"
    "v_mfma_f32_16x16x32_bf16 v[236:239], a[80:83], v[48:51], v[236:239]\n\t"
    "v_cvt_pk_bf16_f32 v202, v204, v205\n\t"
    "v_mul_f32 v206, v206, v245\n\t"
    "v_mul_f32 v207, v207, v245\n\t"
    "v_cvt_pk_bf16_f32 v203, v206, v207\n\t"
    "v_mfma_f32_16x16x32_bf16 v[96:99], v[176:179], v[192:195], v[96:99]\n\t"
    "v_cvt_pk_bf16_f32 v132, v216, v217\n\t"
    "v_cvt_pk_bf16_f32 v133, v218, v219\n\t"
    "v_mfma_f32_16x16x32_bf16 v[80:83], v[176:179], v[200:203], v[80:83]\n\t"
    "v_cvt_pk_bf16_f32 v134, v220, v221\n\t"
    "v_cvt_pk_bf16_f32 v135, v222, v223\n\t"
    "v_mfma_f32_16x16x32_bf16 v[84:87], v[180:183], v[200:203], v[84:87]\n\t"
    "v_cvt_pk_bf16_f32 v152, v224, v225\n\t"
    "v_cvt_pk_bf16_f32 v153, v226, v227\n\t"
    "v_mfma_f32_16x16x32_bf16 v[100:103], v[180:183], v[192:195], v[100:103]\n\t"
    "v_cvt_pk_bf16_f32 v144, v228, v229\n\t"
    "v_cvt_pk_bf16_f32 v145, v230, v231\n\t"
    "v_mfma_f32_16x16x32_bf16 v[104:107], v[184:187], v[192:195], v[104:107]\n\t"
    "v_cvt_pk_bf16_f32 v146, v232, v233\n\t"
    "v_cvt_pk_bf16_f32 v147, v234, v235\n\t"
    "v_mfma_f32_16x16x32_bf16 v[88:91], v[184:187], v[200:203], v[88:91]\n\t"
    "v_cvt_pk_bf16_f32 v154, v236, v237\n\t"
    "v_cvt_pk_bf16_f32 v155, v238, v239\n\t"
    "v_mfma_f32_16x16x32_bf16 v[92:95], v[188:191], v[200:203], v[92:95]\n\t"
    "v_mov_b32_dpp v112, v128 quad_perm:[0,1,2,3] row_mask:0xf bank_mask:0x3\n\t"
    "v_mov_b32_dpp v113, v129 quad_perm:[0,1,2,3] row_mask:0xf bank_mask:0x3\n\t"
    "v_mfma_f32_16x16x32_bf16 v[108:111], v[188:191], v[192:195], v[108:111]\n\t"
    "v_mov_b32_dpp v114, v130 quad_perm:[0,1,2,3] row_mask:0xf bank_mask:0x3\n\t"
    "v_mov_b32_dpp v115, v131 quad_perm:[0,1,2,3] row_mask:0xf bank_mask:0x3\n\t"
    "v_mfma_f32_16x16x32_bf16 v[216:219], a[96:99], v[48:51], 0\n\t"
    "v_mov_b32_dpp v136, v152 quad_perm:[0,1,2,3] row_mask:0xf bank_mask:0x3\n\t"
    "v_mov_b32_dpp v137, v153 quad_perm:[0,1,2,3] row_mask:0xf bank_mask:0x3\n\t"
    "v_mfma_f32_16x16x32_bf16 v[216:219], a[100:103], v[52:55], v[216:219]\n\t"
    "v_mov_b32_dpp v138, v154 quad_perm:[0,1,2,3] row_mask:0xf bank_mask:0x3\n\t"
    "v_mov_b32_dpp v139, v155 quad_perm:[0,1,2,3] row_mask:0xf bank_mask:0x3\n\t"
    "v_mfma_f32_16x16x32_bf16 v[216:219], a[104:107], v[56:59], v[216:219]\n\t"
    "v_cvt_pk_bf16_f32 v96, v96, v97\n\t"
    "v_cvt_pk_bf16_f32 v104, v104, v105\n\t"
    "v_mfma_f32_16x16x32_bf16 v[216:219], a[108:111], v[60:63], v[216:219]\n\t"
    "v_mov_b32_dpp v116, v132 quad_perm:[0,1,2,3] row_mask:0xf bank_mask:0x3\n\t"
    "v_mov_b32_dpp v117, v133 quad_perm:[0,1,2,3] row_mask:0xf bank_mask:0x3\n\t"
    "v_mfma_f32_16x16x32_bf16 v[220:223], a[108:111], v[44:47], 0\n\t"
    "v_mov_b32_dpp v118, v134 quad_perm:[0,1,2,3] row_mask:0xf bank_mask:0x3\n\t"
    "v_mov_b32_dpp v119, v135 quad_perm:[0,1,2,3] row_mask:0xf bank_mask:0x3\n\t"
    "v_mfma_f32_16x16x32_bf16 v[220:223], a[104:107], v[40:43], v[220:223]\n\t"
    "v_cvt_pk_bf16_f32 v97, v98, v99\n\t"
    "v_cvt_pk_bf16_f32 v105, v106, v107\n\t"
    "v_mfma_f32_16x16x32_bf16 v[220:223], a[100:103], v[36:39], v[220:223]\n\t"
    "v_cvt_pk_bf16_f32 v98, v100, v101\n\t"
    "v_cvt_pk_bf16_f32 v106, v108, v109\n\t"
    "v_mfma_f32_16x16x32_bf16 v[220:223], a[96:99], v[32:35], v[220:223]\n\t"
    "v_cvt_pk_bf16_f32 v156, v216, v217\n\t"
    "v_cvt_pk_bf16_f32 v157, v218, v219\n\t"
    "v_mfma_f32_16x16x32_bf16 v[224:227], a[112:115], v[32:35], 0\n\t"
    "v_mov_b32_dpp v140, v156 quad_perm:[0,1,2,3] row_mask:0xf bank_mask:0x3\n\t"
    "v_mov_b32_dpp v141, v157 quad_perm:[0,1,2,3] row_mask:0xf bank_mask:0x3\n\t"
    "v_mfma_f32_16x16x32_bf16 v[224:227], a[116:119], v[36:39], v[224:227]\n\t"
    "v_cvt_pk_bf16_f32 v99, v102, v103\n\t"
    "v_cvt_pk_bf16_f32 v107, v110, v111\n\t"
    "v_mfma_f32_16x16x32_bf16 v[224:227], a[120:123], v[40:43], v[224:227]\n\t"
    "v_pk_max_i16 v96, v96, 0\n\t"
    "v_pk_max_i16 v97, v97, 0\n\t"
    "v_mfma_f32_16x16x32_bf16 v[224:227], a[124:127], v[44:47], v[224:227]\n\t"
    "v_cvt_pk_bf16_f32 v148, v220, v221\n\t"
    "v_cvt_pk_bf16_f32 v149, v222, v223\n\t"
    "v_mfma_f32_16x16x32_bf16 v[228:231], a[124:127], v[60:63], 0\n\t"
    "v_pk_max_i16 v98, v98, 0\n\t"
    "v_pk_max_i16 v99, v99, 0\n\t"
    "v_mfma_f32_16x16x32_bf16 v[228:231], a[120:123], v[56:59], v[228:231]\n\t"
    "v_pk_max_i16 v104, v104, 0\n\t"
    "v_pk_max_i16 v105, v105, 0\n\t"
    "v_mfma_f32_16x16x32_bf16 v[228:231], a[116:119], v[52:55], v[228:231]\n\t"
    "v_pk_max_i16 v106, v106, 0\n\t"
    "v_pk_max_i16 v107, v107, 0\n\t"
    "v_mfma_f32_16x16x32_bf16 v[228:231], a[112:115], v[48:51], v[228:231]\n\t"
    "v_cvt_pk_bf16_f32 v150, v224, v225\n\t"
    "v_cvt_pk_bf16_f32 v151, v226, v227\n\t"
    "v_mfma_f32_16x16x32_bf16 v[232:235], v[32:35], a[128:131], 0\n\t"
    "v_cndmask_b32_dpp v108, v104, v96, vcc row_ror:8 row_mask:0xf bank_mask:0xf\n\t"
    "v_cndmask_b32_dpp v109, v105, v97, vcc row_ror:8 row_mask:0xf bank_mask:0xf\n\t"
    "v_mfma_f32_16x16x32_bf16 v[232:235], v[36:39], a[132:135], v[232:235]\n\t"
    "v_cndmask_b32_dpp v110, v106, v98, vcc row_ror:8 row_mask:0xf bank_mask:0xf\n\t"
    "v_cndmask_b32_dpp v111, v107, v99, vcc row_ror:8 row_mask:0xf bank_mask:0xf\n\t"
    "v_mfma_f32_16x16x32_bf16 v[232:235], v[40:43], a[136:139], v[232:235]\n\t"
    "buffer_store_dwordx4 v[108:111], v15, s[24:27], s49 offen offset:0" STPOL "\n\t"
    "v_mfma_f32_16x16x32_bf16 v[232:235], v[44:47], a[140:143], v[232:235]\n\t"
    "v_cvt_pk_bf16_f32 v158, v228, v229\n\t"
    "v_cvt_pk_bf16_f32 v159, v230, v231\n\t"
    "v_mfma_f32_16x16x32_bf16 v[236:239], v[44:47], a[156:159], 0\n\t"
    "v_mov_b32_dpp v142, v158 quad_perm:[0,1,2,3] row_mask:0xf bank_mask:0x3\n\t"
    "v_mov_b32_dpp v143, v159 quad_perm:[0,1,2,3] row_mask:0xf bank_mask:0x3\n\t"
    "v_mfma_f32_16x16x32_bf16 v[236:239], v[40:43], a[152:155], v[236:239]\n\t"
    "v_mov_b32_dpp v104, v96 row_ror:8 row_mask:0xf bank_mask:0x3\n\t"
    "v_mov_b32_dpp v105, v97 row_ror:8 row_mask:0xf bank_mask:0x3\n\t"
    "v_mfma_f32_16x16x32_bf16 v[236:239], v[36:39], a[148:151], v[236:239]\n\t"
    "v_mov_b32_dpp v106, v98 row_ror:8 row_mask:0xf bank_mask:0x3\n\t"
    "v_mov_b32_dpp v107, v99 row_ror:8 row_mask:0xf bank_mask:0x3\n\t"
    "v_mfma_f32_16x16x32_bf16 v[236:239], v[32:35], a[144:147], v[236:239]\n\t"
    "v_cvt_pk_bf16_f32 v176, v232, v233\n\t"
    "v_cvt_pk_bf16_f32 v177, v234, v235\n\t"
    "v_mfma_f32_16x16x32_bf16 v[192:195], v[144:147], v[112:115], 0\n\t"
    "v_mfma_f32_16x16x32_bf16 v[200:203], v[144:147], v[120:123], 0\n\t"
    "v_mfma_f32_16x16x32_bf16 v[204:207], v[136:139], v[120:123], v[212:215]\n\t"
    "v_mfma_f32_16x16x32_bf16 v[196:199], v[136:139], v[112:115], v[212:215]\n\t"
    "v_cvt_pk_bf16_f32 v180, v236, v237\n\t"
    "v_cvt_pk_bf16_f32 v181, v238, v239\n\t"
    "v_mfma_f32_16x16x32_bf16 v[192:195], v[148:151], v[116:119], v[192:195]\n\t"
    "v_mfma_f32_16x16x32_bf16 v[200:203], v[148:151], v[124:127], v[200:203]\n\t"
    "v_mfma_f32_16x16x32_bf16 v[204:207], v[140:143], v[124:127], v[204:207]\n\t"
    "buffer_store_dwordx4 v[104:107], v12, s[24:27], s66 offen offset:2048" STPOL "\n\t"
    "v_mfma_f32_16x16x32_bf16 v[196:199], v[140:143], v[116:119], v[196:199]\n\t"
    "v_mfma_f32_16x16x32_bf16 v[64:67], a[192:195], v[32:35], 0\n\t"
    "ds_read_b128 v[208:211], v13 offset:576\n\t"
    "v_max3_f32 v240, v192, v193, v194\n\t"
    "v_mfma_f32_16x16x32_bf16 v[64:67], a[196:199], v[36:39], v[64:67]\n\t"
    "v_max3_f32 v241, v200, v201, v202\n\t"
    "v_mfma_f32_16x16x32_bf16 v[64:67], a[200:203], v[40:43], v[64:67]\n\t"
    "v_max3_f32 v243, v204, v205, v206\n\t"
    "v_max3_f32 v241, v241, v203, v207\n\t"
    "v_mfma_f32_16x16x32_bf16 v[64:67], a[204:207], v[44:47], v[64:67]\n\t"
    "v_max3_f32 v242, v196, v197, v198\n\t"
    "v_max3_f32 v240, v240, v195, v199\n\t"
    "v_mfma_f32_16x16x32_bf16 v[68:71], a[220:223], v[44:47], 0\n\t"
    "v_max_f32 v240, v240, v242\n\t"
    "v_max_f32 v241, v241, v243\n\t"
    "v_mfma_f32_16x16x32_bf16 v[68:71], a[216:219], v[40:43], v[68:71]\n\t"
    "s_nop 0\n\t"
    "v_permlane32_swap_b32 v240, v241\n\t"
    "v_mfma_f32_16x16x32_bf16 v[68:71], a[212:215], v[36:39], v[68:71]\n\t"
    "v_max_f32 v240, v240, v241\n\t"
    "v_mov_b32 v242, v240\n\t"
    "v_mfma_f32_16x16x32_bf16 v[68:71], a[208:211], v[32:35], v[68:71]\n\t"
    "s_nop 0\n\t"
    "v_permlane16_swap_b32 v240, v242\n\t"
    "v_mfma_f32_16x16x32_bf16 v[72:75], a[224:227], v[32:35], 0\n\t"
    "v_max_f32 v240, v240, v242\n\t"
    "v_mul_f32 v241, 0xbe38aa3b, v240\n\t"
    "v_mfma_f32_16x16x32_bf16 v[72:75], a[228:231], v[36:39], v[72:75]\n\t"
    "v_mul_f32 v240, 0xbe38aa3b, v240\n\t"
    "v_mfma_f32_16x16x32_bf16 v[72:75], a[232:235], v[40:43], v[72:75]\n\t"
    "s_nop 0\n\t"
    "v_permlane32_swap_b32 v240, v241\n\t"
    "v_mfma_f32_16x16x32_bf16 v[72:75], a[236:239], v[44:47], v[72:75]\n\t"
    "v_fmamk_f32 v192, v192, 0x3e38aa3b, v240\n\t"
    "v_fmamk_f32 v193, v193, 0x3e38aa3b, v240\n\t"
    "v_mfma_f32_16x16x32_bf16 v[76:79], a[252:255], v[44:47], 0\n\t"
    "v_fmamk_f32 v194, v194, 0x3e38aa3b, v240\n\t"
    "v_fmamk_f32 v195, v195, 0x3e38aa3b, v240\n\t"
    "v_mfma_f32_16x16x32_bf16 v[76:79], a[248:251], v[40:43], v[76:79]\n\t"
    "v_fmamk_f32 v200, v200, 0x3e38aa3b, v241\n\t"
    "v_fmamk_f32 v201, v201, 0x3e38aa3b, v241\n\t"
    "v_mfma_f32_16x16x32_bf16 v[76:79], a[244:247], v[36:39], v[76:79]\n\t"
    "v_fmamk_f32 v202, v202, 0x3e38aa3b, v241\n\t"
    "v_fmamk_f32 v203, v203, 0x3e38aa3b, v241\n\t"
    "v_mfma_f32_16x16x32_bf16 v[76:79], a[240:243], v[32:35], v[76:79]\n\t"
    "v_exp_f32 v192, v192\n\t"
    "v_mfma_f32_16x16x32_bf16 v[216:219], v[32:35], a[160:163], 0\n\t"
    "v_exp_f32 v193, v193\n\t"
    "v_mfma_f32_16x16x32_bf16 v[216:219], v[36:39], a[164:167], v[216:219]\n\t"
    "v_exp_f32 v194, v194\n\t"
    "v_mfma_f32_16x16x32_bf16 v[216:219], v[40:43], a[168:171], v[216:219]\n\t"
    "v_exp_f32 v195, v195\n\t"
    "v_mfma_f32_16x16x32_bf16 v[216:219], v[44:47], a[172:175], v[216:219]\n\t"
    "s_waitcnt vmcnt(10)\n\t"
    "v_fmamk_f32 v196, v196, 0x3e38aa3b, v240\n\t"
    "v_mfma_f32_16x16x32_bf16 v[220:223], v[44:47], a[188:191], 0\n\t"
    "ds_read_b128 v[44:47], v11 offset:17600\n\t"
    "v_fmamk_f32 v197, v197, 0x3e38aa3b, v240\n\t"
    "v_mfma_f32_16x16x32_bf16 v[220:223], v[40:43], a[184:187], v[220:223]\n\t"
    "ds_read_b128 v[40:43], v11 offset:17536\n\t"
    "v_fmamk_f32 v204, v204, 0x3e38aa3b, v241\n\t"
    "v_mfma_f32_16x16x32_bf16 v[220:223], v[36:39], a[180:183], v[220:223]\n\t"
    ".Ls3_%=:\n\t"
    "s_cbranch_execz .Lret%=\n\t"
    "ds_read_b128 v[36:39], v11 offset:17472\n\t"
    "v_mfma_f32_16x16x32_bf16 v[220:223], v[32:35], a[176:179], v[220:223]\n\t"
    "ds_read_b128 v[32:35], v11 offset:17408\n\t"
    "v_cvt_pk_bf16_f32 v184, v216, v217\n\t"
    "v_mfma_f32_16x16x32_bf16 v[224:227], v[48:51], a[128:131], 0\n\t"
    "v_cvt_pk_bf16_f32 v185, v218, v219\n\t"
    "v_fmamk_f32 v205, v205, 0x3e38aa3b, v241\n\t"
    "v_mfma_f32_16x16x32_bf16 v[224:227], v[52:55], a[132:135], v[224:227]\n\t"
    "v_exp_f32 v200, v200\n\t"
    "v_mfma_f32_16x16x32_bf16 v[224:227], v[56:59], a[136:139], v[224:227]\n\t"
    "v_exp_f32 v201, v201\n\t"
    "v_mfma_f32_16x16x32_bf16 v[224:227], v[60:63], a[140:143], v[224:227]\n\t"
    "v_cvt_pk_bf16_f32 v188, v220, v221\n\t"
    "v_cvt_pk_bf16_f32 v189, v222, v223\n\t"
    "v_mfma_f32_16x16x32_bf16 v[228:231], v[60:63], a[156:159], 0\n\t"
    "v_exp_f32 v202, v202\n\t"
    "v_mfma_f32_16x16x32_bf16 v[228:231], v[56:59], a[152:155], v[228:231]\n\t"
    "v_exp_f32 v203, v203\n\t"
    "v_mfma_f32_16x16x32_bf16 v[228:231], v[52:55], a[148:151], v[228:231]\n\t"
    "v_exp_f32 v196, v196\n\t"
    "v_mfma_f32_16x16x32_bf16 v[228:231], v[48:51], a[144:147], v[228:231]\n\t"
    "s_waitcnt lgkmcnt(0)\n\t"
    "v_cvt_pk_bf16_f32 v160, v224, v225\n\t"
    "v_mfma_f32_16x16x32_bf16 v[232:235], v[48:51], a[160:163], 0\n\t"
    "v_cvt_pk_bf16_f32 v161, v226, v227\n\t"
    "v_fmamk_f32 v198, v198, 0x3e38aa3b, v240\n\t"
    "v_mfma_f32_16x16x32_bf16 v[232:235], v[52:55], a[164:167], v[232:235]\n\t"
    "v_exp_f32 v197, v197\n\t"
    "v_mfma_f32_16x16x32_bf16 v[232:235], v[56:59], a[168:171], v[232:235]\n\t"
    "v_exp_f32 v204, v204\n\t"
    "v_mfma_f32_16x16x32_bf16 v[232:235], v[60:63], a[172:175], v[232:235]\n\t"
    "v_cvt_pk_bf16_f32 v164, v228, v229\n\t"
    "v_cvt_pk_bf16_f32 v165, v230, v231\n\t"
    "v_mfma_f32_16x16x32_bf16 v[236:239], v[60:63], a[188:191], 0\n\t"
    "v_exp_f32 v205, v205\n\t"
    "v_mfma_f32_16x16x32_bf16 v[236:239], v[56:59], a[184:187], v[236:239]\n\t"
    "v_fmamk_f32 v199, v199, 0x3e38aa3b, v240\n\t"
    "v_fmamk_f32 v206, v206, 0x3e38aa3b, v241\n\t"
    "v_mfma_f32_16x16x32_bf16 v[236:239], v[52:55], a[180:183], v[236:239]\n\t"
    "v_fmamk_f32 v207, v207, 0x3e38aa3b, v241\n\t"
    "v_add_f32 v244, v192, v193\n\t"
    "v_mfma_f32_16x16x32_bf16 v[236:239], v[48:51], a[176:179], v[236:239]\n\t"
    "v_cvt_pk_bf16_f32 v168, v232, v233\n\t"
    "v_cvt_pk_bf16_f32 v169, v234, v235\n\t"
    "v_mfma_f32_16x16x32_bf16 v[216:219], a[0:3], v[32:35], 0\n\t"
    "v_add_f32 v246, v194, v195\n\t"
    "v_add_f32 v245, v200, v201\n\t"
    "v_mfma_f32_16x16x32_bf16 v[216:219], a[4:7], v[36:39], v[216:219]\n\t"
    "v_add_f32 v247, v202, v203\n\t"
    "v_add_f32 v244, v244, v196\n\t"
    "v_mfma_f32_16x16x32_bf16 v[216:219], a[8:11], v[40:43], v[216:219]\n\t"
    "v_exp_f32 v198, v198\n\t"
    "v_mfma_f32_16x16x32_bf16 v[216:219], a[12:15], v[44:47], v[216:219]\n\t"
    "v_exp_f32 v199, v199\n\t"
    "v_mfma_f32_16x16x32_bf16 v[220:223], a[28:31], v[44:47], 0\n\t"
    "v_exp_f32 v206, v206\n\t"
    "v_mfma_f32_16x16x32_bf16 v[220:223], a[24:27], v[40:43], v[220:223]\n\t"
    "v_exp_f32 v207, v207\n\t"
    "v_mfma_f32_16x16x32_bf16 v[220:223], a[20:23], v[36:39], v[220:223]\n\t"
    "v_add_f32 v246, v246, v197\n\t"
    "v_add_f32 v245, v245, v204\n\t"
    "v_mfma_f32_16x16x32_bf16 v[220:223], a[16:19], v[32:35], v[220:223]\n\t"
    "v_add_f32 v247, v247, v205\n\t"
    "v_add_f32 v244, v244, v198\n\t"
    "v_mfma_f32_16x16x32_bf16 v[224:227], a[32:35], v[32:35], 0\n\t"
    "v_add_f32 v246, v246, v199\n\t"
    "v_add_f32 v245, v245, v206\n\t"
    "v_mfma_f32_16x16x32_bf16 v[224:227], a[36:39], v[36:39], v[224:227]\n\t"
    "v_add_f32 v247, v247, v207\n\t"
    "v_add_f32 v244, v244, v246\n\t"
    "v_mfma_f32_16x16x32_bf16 v[224:227], a[40:43], v[40:43], v[224:227]\n\t"
    "v_add_f32 v245, v245, v247\n\t"
    "v_cvt_pk_bf16_f32 v172, v236, v237\n\t"
    "v_mfma_f32_16x16x32_bf16 v[224:227], a[44:47], v[44:47], v[224:227]\n\t"
    "v_cvt_pk_bf16_f32 v173, v238, v239\n\t"
    "v_cvt_pk_bf16_f32 v112, v216, v217\n\t"
    "v_mfma_f32_16x16x32_bf16 v[228:231], a[60:63], v[44:47], 0\n\t"
    "v_permlane32_swap_b32 v244, v245\n\t"
    "v_mfma_f32_16x16x32_bf16 v[228:231], a[56:59], v[40:43], v[228:231]\n\t"
    "v_add_f32 v244, v244, v245\n\t"
    "v_mov_b32 v246, v244\n\t"
    "v_mfma_f32_16x16x32_bf16 v[228:231], a[52:55], v[36:39], v[228:231]\n\t"
    "s_nop 0\n\t"
    "v_permlane16_swap_b32 v244, v246\n\t"
    "v_mfma_f32_16x16x32_bf16 v[228:231], a[48:51], v[32:35], v[228:231]\n\t"
    "v_add_f32 v244, v244, v246\n\t"
    "v_cvt_pk_bf16_f32 v113, v218, v219\n\t"
    "v_mfma_f32_16x16x32_bf16 v[232:235], a[64:67], v[32:35], 0\n\t"
    "v_rcp_f32 v244, v244\n\t"
    "v_mfma_f32_16x16x32_bf16 v[232:235], a[68:71], v[36:39], v[232:235]\n\t"
    "v_mov_b32 v245, v244\n\t"
    "v_cvt_pk_bf16_f32 v114, v220, v221\n\t"
    "v_mfma_f32_16x16x32_bf16 v[232:235], a[72:75], v[40:43], v[232:235]\n\t"
    "v_permlane32_swap_b32 v244, v245\n\t"
    "v_mfma_f32_16x16x32_bf16 v[232:235], a[76:79], v[44:47], v[232:235]\n\t"
    "v_mul_f32 v192, v192, v244\n\t"
    "v_mul_f32 v193, v193, v244\n\t"
    "v_mfma_f32_16x16x32_bf16 v[236:239], a[92:95], v[44:47], 0\n\t"
    "v_cvt_pk_bf16_f32 v115, v222, v223\n\t"
    "v_cvt_pk_bf16_f32 v192, v192, v193\n\t"
    "v_mfma_f32_16x16x32_bf16 v[236:239], a[88:91], v[40:43], v[236:239]\n\t"
    "v_mul_f32 v194, v194, v244\n\t"
    "v_mul_f32 v195, v195, v244\n\t"
    "v_mfma_f32_16x16x32_bf16 v[236:239], a[84:87], v[36:39], v[236:239]\n\t"
    "v_cvt_pk_bf16_f32 v193, v194, v195\n\t"
    "v_mul_f32 v196, v196, v244\n\t"
    "v_mfma_f32_16x16x32_bf16 v[236:239], a[80:83], v[32:35], v[236:239]\n\t"
    "v_mul_f32 v197, v197, v244\n\t"
    "v_cvt_pk_bf16_f32 v194, v196, v197\n\t"
    "v_mfma_f32_16x16x32_bf16 v[216:219], a[96:99], v[32:35], 0\n\t"
    "v_mul_f32 v198, v198, v244\n\t"
    "v_mul_f32 v199, v199, v244\n\t"
    "v_mfma_f32_16x16x32_bf16 v[216:219], a[100:103], v[36:39], v[216:219]\n\t"
    "v_cvt_pk_bf16_f32 v195, v198, v199\n\t"
    "v_cndmask_b32 v196, 0, v192, s[54:55]\n\t"
    "v_mfma_f32_16x16x32_bf16 v[216:219], a[104:107], v[40:43], v[216:219]\n\t"
    "v_cndmask_b32 v197, 0, v193, s[54:55]\n\t"
    "v_cndmask_b32 v198, 0, v194, s[54:55]\n\t"
    "v_mfma_f32_16x16x32_bf16 v[216:219], a[108:111], v[44:47], v[216:219]\n\t"
    "v_cndmask_b32 v199, 0, v195, s[54:55]\n\t"
    "v_mov_b32_dpp v178, v160 quad_perm:[0,1,2,3] row_mask:0x3 bank_mask:0xf\n\t"
    "v_mfma_f32_16x16x32_bf16 v[220:223], a[124:127], v[44:47], 0\n\t"
    "v_mov_b32_dpp v179, v161 quad_perm:[0,1,2,3] row_mask:0x3 bank_mask:0xf\n\t"
    "v_cndmask_b32 v192, v192, 0, s[60:61]\n\t"
    "v_mfma_f32_16x16x32_bf16 v[220:223], a[120:123], v[40:43], v[220:223]\n\t"
    "v_cndmask_b32 v193, v193, 0, s[60:61]\n\t"
    "v_cndmask_b32 v194, v194, 0, s[60:61]\n\t"
    "v_mfma_f32_16x16x32_bf16 v[220:223], a[116:119], v[36:39], v[220:223]\n\t"
    "v_cndmask_b32 v195, v195, 0, s[60:61]\n\t"
    "v_mul_f32 v200, v200, v245\n\t"
    "v_mul_f32 v201, v201, v245\n\t"
    "v_mfma_f32_16x16x32_bf16 v[220:223], a[112:115], v[32:35], v[220:223]\n\t"
    "v_cvt_pk_bf16_f32 v200, v200, v201\n\t"
    "v_mul_f32 v202, v202, v245\n\t"
    "v_mul_f32 v203, v203, v245\n\t"
    "v_cvt_pk_bf16_f32 v201, v202, v203\n\t"
    "v_mul_f32 v204, v204, v245\n\t"
    "v_mul_f32 v205, v205, v245\n\t"
    "v_mfma_f32_16x16x32_bf16 v[80:83], v[176:179], v[192:195], v[80:83]\n\t"
    "v_cvt_pk_bf16_f32 v202, v204, v205\n\t"
    "v_mul_f32 v206, v206, v245\n\t"
    "v_mul_f32 v207, v207, v245\n\t"
    "v_cvt_pk_bf16_f32 v203, v206, v207\n\t"
    "v_mfma_f32_16x16x32_bf16 v[96:99], v[176:179], v[196:199], 0\n\t"
    "v_mov_b32_dpp v182, v164 quad_perm:[0,1,2,3] row_mask:0x3 bank_mask:0xf\n\t"
    "v_mov_b32_dpp v183, v165 quad_perm:[0,1,2,3] row_mask:0x3 bank_mask:0xf\n\t"
    "v_mfma_f32_16x16x32_bf16 v[64:67], v[176:179], v[200:203], v[64:67]\n\t"
    "v_mov_b32_dpp v186, v168 quad_perm:[0,1,2,3] row_mask:0x3 bank_mask:0xf\n\t"
    "v_mov_b32_dpp v187, v169 quad_perm:[0,1,2,3] row_mask:0x3 bank_mask:0xf\n\t"
    "v_mfma_f32_16x16x32_bf16 v[68:71], v[180:183], v[200:203], v[68:71]\n\t"
    "v_mov_b32_dpp v190, v172 quad_perm:[0,1,2,3] row_mask:0x3 bank_mask:0xf\n\t"
    "v_mov_b32_dpp v191, v173 quad_perm:[0,1,2,3] row_mask:0x3 bank_mask:0xf\n\t"
    "v_mfma_f32_16x16x32_bf16 v[100:103], v[180:183], v[196:199], 0\n\t"
    "v_cvt_pk_bf16_f32 v136, v232, v233\n\t"
    "v_cvt_pk_bf16_f32 v137, v234, v235\n\t"
    "v_mfma_f32_16x16x32_bf16 v[84:87], v[180:183], v[192:195], v[84:87]\n\t"
    "v_cvt_pk_bf16_f32 v138, v236, v237\n\t"
    "v_cvt_pk_bf16_f32 v139, v238, v239\n\t"
    "v_mfma_f32_16x16x32_bf16 v[88:91], v[184:187], v[192:195], v[88:91]\n\t"
    "v_cvt_pk_bf16_f32 v116, v224, v225\n\t"
    "v_cvt_pk_bf16_f32 v117, v226, v227\n\t"
    "v_mfma_f32_16x16x32_bf16 v[104:107], v[184:187], v[196:199], 0\n\t"
    "v_cvt_pk_bf16_f32 v118, v228, v229\n\t"
    "v_cvt_pk_bf16_f32 v119, v230, v231\n\t"
    "v_mfma_f32_16x16x32_bf16 v[72:75], v[184:187], v[200:203], v[72:75]\n\t"
    "v_cvt_pk_bf16_f32 v140, v216, v217\n\t"
    "v_cvt_pk_bf16_f32 v141, v218, v219\n\t"
    "v_mfma_f32_16x16x32_bf16 v[76:79], v[188:191], v[200:203], v[76:79]\n\t"
    "v_cvt_pk_bf16_f32 v142, v220, v221\n\t"
    "v_cvt_pk_bf16_f32 v143, v222, v223\n\t"
    "v_mfma_f32_16x16x32_bf16 v[108:111], v[188:191], v[196:199], 0\n\t"
    "v_cvt_pk_bf16_f32 v64, v64, v65\n\t"
    "v_cvt_pk_bf16_f32 v65, v66, v67\n\t"
    "v_mfma_f32_16x16x32_bf16 v[92:95], v[188:191], v[192:195], v[92:95]\n\t"
    "v_cvt_pk_bf16_f32 v66, v68, v69\n\t"
    "v_cvt_pk_bf16_f32 v67, v70, v71\n\t"
    "v_mfma_f32_16x16x32_bf16 v[192:195], v[152:155], v[128:131], v[208:211]\n\t"
    "v_cvt_pk_bf16_f32 v72, v72, v73\n\t"
    "v_cvt_pk_bf16_f32 v73, v74, v75\n\t"
    "v_mfma_f32_16x16x32_bf16 v[200:203], v[152:155], v[112:115], v[208:211]\n\t"
    "v_cvt_pk_bf16_f32 v74, v76, v77\n\t"
    "v_cvt_pk_bf16_f32 v75, v78, v79\n\t"
    "v_mfma_f32_16x16x32_bf16 v[204:207], v[136:139], v[112:115], 0\n\t"
    "v_pk_max_i16 v64, v64, 0\n\t"
    "v_pk_max_i16 v65, v65, 0\n\t"
    "v_mfma_f32_16x16x32_bf16 v[196:199], v[136:139], v[128:131], 0\n\t"
    "v_pk_max_i16 v66, v66, 0\n\t"
    "v_pk_max_i16 v67, v67, 0\n\t"
    "v_mfma_f32_16x16x32_bf16 v[192:195], v[156:159], v[132:135], v[192:195]\n\t"
    "v_pk_max_i16 v72, v72, 0\n\t"
    "v_pk_max_i16 v73, v73, 0\n\t"
    "v_mfma_f32_16x16x32_bf16 v[200:203], v[156:159], v[116:119], v[200:203]\n\t"
    "v_pk_max_i16 v74, v74, 0\n\t"
    "v_pk_max_i16 v75, v75, 0\n\t"
    "v_mfma_f32_16x16x32_bf16 v[204:207], v[140:143], v[116:119], v[204:207]\n\t"
    "v_cvt_pk_bf16_f32 v80, v80, v81\n\t"
    "v_cvt_pk_bf16_f32 v88, v88, v89\n\t"
    "v_mfma_f32_16x16x32_bf16 v[196:199], v[140:143], v[132:135], v[196:199]\n\t"
    "v_cvt_pk_bf16_f32 v81, v82, v83\n\t"
    "v_cvt_pk_bf16_f32 v89, v90, v91\n\t"
    "v_mfma_f32_16x16x32_bf16 v[224:227], v[32:35], a[128:131], 0\n\t"
    "v_cndmask_b32_dpp v76, v72, v64, vcc row_ror:8 row_mask:0xf bank_mask:0xf\n\t"
    "v_cndmask_b32_dpp v77, v73, v65, vcc row_ror:8 row_mask:0xf bank_mask:0xf\n\t"
    "v_mfma_f32_16x16x32_bf16 v[224:227], v[36:39], a[132:135], v[224:227]\n\t"
    "v_cndmask_b32_dpp v78, v74, v66, vcc row_ror:8 row_mask:0xf bank_mask:0xf\n\t"
    "v_cndmask_b32_dpp v79, v75, v67, vcc row_ror:8 row_mask:0xf bank_mask:0xf\n\t"
    "v_mfma_f32_16x16x32_bf16 v[224:227], v[40:43], a[136:139], v[224:227]\n\t"
    "buffer_store_dwordx4 v[76:79], v12, s[24:27], s69 offen offset:0" STPOL "\n\t"
    "v_mfma_f32_16x16x32_bf16 v[224:227], v[44:47], a[140:143], v[224:227]\n\t"
    "v_cvt_pk_bf16_f32 v82, v84, v85\n\t"
    "v_cvt_pk_bf16_f32 v90, v92, v93\n\t"
    "v_mfma_f32_16x16x32_bf16 v[228:231], v[44:47], a[156:159], 0\n\t"
    "v_mov_b32_dpp v72, v64 row_ror:8 row_mask:0xf bank_mask:0x3\n\t"
    "v_mov_b32_dpp v73, v65 row_ror:8 row_mask:0xf bank_mask:0x3\n\t"
    "v_mfma_f32_16x16x32_bf16 v[228:231], v[40:43], a[152:155], v[228:231]\n\t"
    "v_mov_b32_dpp v74, v66 row_ror:8 row_mask:0xf bank_mask:0x3\n\t"
    "v_mov_b32_dpp v75, v67 row_ror:8 row_mask:0xf bank_mask:0x3\n\t"
    "v_mfma_f32_16x16x32_bf16 v[228:231], v[36:39], a[148:151], v[228:231]\n\t"
    "v_cvt_pk_bf16_f32 v83, v86, v87\n\t"
    "v_cvt_pk_bf16_f32 v91, v94, v95\n\t"
    "v_mfma_f32_16x16x32_bf16 v[228:231], v[32:35], a[144:147], v[228:231]\n\t"
    "v_pk_max_i16 v80, v80, 0\n\t"
    "v_pk_max_i16 v81, v81, 0\n\t"
    "v_mfma_f32_16x16x32_bf16 v[232:235], v[32:35], a[160:163], 0\n\t"
    "v_pk_max_i16 v82, v82, 0\n\t"
    "v_pk_max_i16 v83, v83, 0\n\t"
    "v_mfma_f32_16x16x32_bf16 v[232:235], v[36:39], a[164:167], v[232:235]\n\t"
    "v_pk_max_i16 v88, v88, 0\n\t"
    "v_pk_max_i16 v89, v89, 0\n\t"
    "v_mfma_f32_16x16x32_bf16 v[232:235], v[40:43], a[168:171], v[232:235]\n\t"
    "v_pk_max_i16 v90, v90, 0\n\t"
    "v_pk_max_i16 v91, v91, 0\n\t"
    "v_mfma_f32_16x16x32_bf16 v[232:235], v[44:47], a[172:175], v[232:235]\n\t"
    "v_cndmask_b32_dpp v92, v88, v80, vcc row_ror:8 row_mask:0xf bank_mask:0xf\n\t"
    "v_cndmask_b32_dpp v93, v89, v81, vcc row_ror:8 row_mask:0xf bank_mask:0xf\n\t"
    "v_mfma_f32_16x16x32_bf16 v[236:239], v[44:47], a[188:191], 0\n\t"
    "v_cndmask_b32_dpp v94, v90, v82, vcc row_ror:8 row_mask:0xf bank_mask:0xf\n\t"
    "v_cndmask_b32_dpp v95, v91, v83, vcc row_ror:8 row_mask:0xf bank_mask:0xf\n\t"
    "v_mfma_f32_16x16x32_bf16 v[236:239], v[40:43], a[184:187], v[236:239]\n\t"
    "v_mov_b32_dpp v88, v80 row_ror:8 row_mask:0xf bank_mask:0x3\n\t"
    "v_mov_b32_dpp v89, v81 row_ror:8 row_mask:0xf bank_mask:0x3\n\t"
    "v_mfma_f32_16x16x32_bf16 v[236:239], v[36:39], a[180:183], v[236:239]\n\t"
    "buffer_store_dwordx4 v[72:75], v12, s[24:27], s69 offen offset:2048" STPOL "\n\t"
    "v_mfma_f32_16x16x32_bf16 v[236:239], v[32:35], a[176:179], v[236:239]\n\t"
    "v_mov_b32_dpp v90, v82 row_ror:8 row_mask:0xf bank_mask:0x3\n\t"
    "v_mov_b32_dpp v91, v83 row_ror:8 row_mask:0xf bank_mask:0x3\n\t"
    "v_mfma_f32_16x16x32_bf16 v[96:99], a[192:195], v[48:51], v[96:99]\n\t"
    "v_max3_f32 v240, v192, v193, v194\n\t"
    "v_max3_f32 v241, v200, v201, v202\n\t"
    "v_mfma_f32_16x16x32_bf16 v[96:99], a[196:199], v[52:55], v[96:99]\n\t"
    "v_max3_f32 v243, v204, v205, v206\n\t"
    "v_max3_f32 v241, v241, v203, v207\n\t"
    "v_mfma_f32_16x16x32_bf16 v[96:99], a[200:203], v[56:59], v[96:99]\n\t"
    "v_max3_f32 v242, v196, v197, v198\n\t"
    "v_max3_f32 v240, v240, v195, v199\n\t"
    "v_mfma_f32_16x16x32_bf16 v[96:99], a[204:207], v[60:63], v[96:99]\n\t"
    "v_max_f32 v240, v240, v242\n\t"
    "v_max_f32 v241, v241, v243\n\t"
    "v_mfma_f32_16x16x32_bf16 v[100:103], a[220:223], v[60:63], v[100:103]\n\t"
    "s_nop 0\n\t"
    "v_permlane32_swap_b32 v240, v241\n\t"
    "v_mfma_f32_16x16x32_bf16 v[100:103], a[216:219], v[56:59], v[100:103]\n\t"
    "v_max_f32 v240, v240, v241\n\t"
    "v_mov_b32 v242, v240\n\t"
    "v_mfma_f32_16x16x32_bf16 v[100:103], a[212:215], v[52:55], v[100:103]\n\t"
    "s_nop 0\n\t"
    "v_permlane16_swap_b32 v240, v242\n\t"
    "v_mfma_f32_16x16x32_bf16 v[100:103], a[208:211], v[48:51], v[100:103]\n\t"
    "v_max_f32 v240, v240, v242\n\t"
    "v_mul_f32 v241, 0xbe38aa3b, v240\n\t"
    "v_mfma_f32_16x16x32_bf16 v[104:107], a[224:227], v[48:51], v[104:107]\n\t"
    "v_mul_f32 v240, 0xbe38aa3b, v240\n\t"
    "v_cvt_pk_bf16_f32 v162, v224, v225\n\t"
    "v_mfma_f32_16x16x32_bf16 v[104:107], a[228:231], v[52:55], v[104:107]\n\t"
    "v_permlane32_swap_b32 v240, v241\n\t"
    "v_mfma_f32_16x16x32_bf16 v[104:107], a[232:235], v[56:59], v[104:107]\n\t"
    "buffer_store_dwordx4 v[92:95], v12, s[24:27], s67 offen offset:0" STPOL "\n\t"
    "v_mfma_f32_16x16x32_bf16 v[104:107], a[236:239], v[60:63], v[104:107]\n\t"
    "v_fmamk_f32 v192, v192, 0x3e38aa3b, v240\n\t"
    "v_fmamk_f32 v193, v193, 0x3e38aa3b, v240\n\t"
    "v_mfma_f32_16x16x32_bf16 v[108:111], a[252:255], v[60:63], v[108:111]\n\t"
    "v_fmamk_f32 v194, v194, 0x3e38aa3b, v240\n\t"
    "v_fmamk_f32 v195, v195, 0x3e38aa3b, v240\n\t"
    "v_mfma_f32_16x16x32_bf16 v[108:111], a[248:251], v[56:59], v[108:111]\n\t"
    "v_fmamk_f32 v200, v200, 0x3e38aa3b, v241\n\t"
    "v_fmamk_f32 v201, v201, 0x3e38aa3b, v241\n\t"
    "v_mfma_f32_16x16x32_bf16 v[108:111], a[244:247], v[52:55], v[108:111]\n\t"
    "v_fmamk_f32 v202, v202, 0x3e38aa3b, v241\n\t"
    "v_fmamk_f32 v203, v203, 0x3e38aa3b, v241\n\t"
    "v_mfma_f32_16x16x32_bf16 v[108:111], a[240:243], v[48:51], v[108:111]\n\t"
    "v_exp_f32 v192, v192\n\t"
    "v_mfma_f32_16x16x32_bf16 v[64:67], a[192:195], v[32:35], 0\n\t"
    "v_exp_f32 v193, v193\n\t"
    "v_exp_f32 v194, v194\n\t"
    "v_exp_f32 v195, v195\n\t"
    "v_exp_f32 v200, v200\n\t"
    "v_exp_f32 v201, v201\n\t"
    "v_exp_f32 v202, v202\n\t"
    "v_exp_f32 v203, v203\n\t"
    "v_fmamk_f32 v196, v196, 0x3e38aa3b, v240\n\t"
    "v_fmamk_f32 v197, v197, 0x3e38aa3b, v240\n\t"
    "v_fmamk_f32 v204, v204, 0x3e38aa3b, v241\n\t"
    "v_fmamk_f32 v205, v205, 0x3e38aa3b, v241\n\t"
    "v_mfma_f32_16x16x32_bf16 v[64:67], a[196:199], v[36:39], v[64:67]\n\t"
    "v_exp_f32 v196, v196\n\t"
    "v_exp_f32 v197, v197\n\t"
    "v_exp_f32 v204, v204\n\t"
    "v_exp_f32 v205, v205\n\t"
    "v_fmamk_f32 v198, v198, 0x3e38aa3b, v240\n\t"
    "v_fmamk_f32 v199, v199, 0x3e38aa3b, v240\n\t"
    "v_fmamk_f32 v206, v206, 0x3e38aa3b, v241\n\t"
    "v_fmamk_f32 v207, v207, 0x3e38aa3b, v241\n\t"
    "v_add_f32 v244, v192, v193\n\t"
    "v_add_f32 v246, v194, v195\n\t"
    "v_add_f32 v245, v200, v201\n\t"
    "v_add_f32 v247, v202, v203\n\t"
    "v_mfma_f32_16x16x32_bf16 v[64:67], a[200:203], v[40:43], v[64:67]\n\t"
    "v_exp_f32 v198, v198\n\t"
    "v_exp_f32 v199, v199\n\t"
    "v_exp_f32 v206, v206\n\t"
    "v_exp_f32 v207, v207\n\t"
    "v_add_f32 v244, v244, v196\n\t"
    "v_add_f32 v246, v246, v197\n\t"
    "v_add_f32 v245, v245, v204\n\t"
    "v_add_f32 v247, v247, v205\n\t"
    "v_mfma_f32_16x16x32_bf16 v[64:67], a[204:207], v[44:47], v[64:67]\n\t"
    "v_add_f32 v244, v244, v198\n\t"
    "v_add_f32 v246, v246, v199\n\t"
    "v_add_f32 v245, v245, v206\n\t"
    "v_add_f32 v247, v247, v207\n\t"
    "v_add_f32 v244, v244, v246\n\t"
    "v_add_f32 v245, v245, v247\n\t"
    "v_mfma_f32_16x16x32_bf16 v[68:71], a[220:223], v[44:47], 0\n\t"
    "s_nop 0\n\t"
    "v_permlane32_swap_b32 v244, v245\n\t"
    "v_mfma_f32_16x16x32_bf16 v[68:71], a[216:219], v[40:43], v[68:71]\n\t"
    "v_add_f32 v244, v244, v245\n\t"
    "v_mov_b32 v246, v244\n\t"
    "v_mfma_f32_16x16x32_bf16 v[68:71], a[212:215], v[36:39], v[68:71]\n\t"
    "buffer_store_dwordx4 v[88:91], v16, s[24:27], s67 offen offset:2048" STPOL "\n\t"
    "v_mfma_f32_16x16x32_bf16 v[68:71], a[208:211], v[32:35], v[68:71]\n\t"
    "v_permlane16_swap_b32 v244, v246\n\t"
    "v_mfma_f32_16x16x32_bf16 v[72:75], a[224:227], v[32:35], 0\n\t"
    "v_add_f32 v244, v244, v246\n\t"
    "v_cvt_pk_bf16_f32 v163, v226, v227\n\t"
    "v_mfma_f32_16x16x32_bf16 v[72:75], a[228:231], v[36:39], v[72:75]\n\t"
    "v_rcp_f32 v244, v244\n\t"
    "v_mfma_f32_16x16x32_bf16 v[72:75], a[232:235], v[40:43], v[72:75]\n\t"
    "v_mov_b32 v245, v244\n\t"
    "v_cvt_pk_bf16_f32 v166, v228, v229\n\t"
    "v_mfma_f32_16x16x32_bf16 v[72:75], a[236:239], v[44:47], v[72:75]\n\t"
    "v_permlane32_swap_b32 v244, v245\n\t"
    "v_mfma_f32_16x16x32_bf16 v[76:79], a[252:255], v[44:47], 0\n\t"
    "v_cndmask_b32 v244, v244, 0, s[54:55]\n\t"
    "v_mul_f32 v192, v192, v244\n\t"
    "v_mul_f32 v193, v193, v244\n\t"
    "v_mfma_f32_16x16x32_bf16 v[76:79], a[248:251], v[40:43], v[76:79]\n\t"
    "v_cvt_pk_bf16_f32 v192, v192, v193\n\t"
    "v_mul_f32 v194, v194, v244\n\t"
    "v_mul_f32 v195, v195, v244\n\t"
    "v_cvt_pk_bf16_f32 v193, v194, v195\n\t"
    "v_mul_f32 v196, v196, v244\n\t"
    "v_mul_f32 v197, v197, v244\n\t"
    "v_mul_f32 v200, v200, v245\n\t"
    "v_mul_f32 v201, v201, v245\n\t"
    "v_mfma_f32_16x16x32_bf16 v[76:79], a[244:247], v[36:39], v[76:79]\n\t"
    "v_cvt_pk_bf16_f32 v194, v196, v197\n\t"
    "v_mul_f32 v198, v198, v244\n\t"
    "v_mul_f32 v199, v199, v244\n\t"
    "v_cvt_pk_bf16_f32 v200, v200, v201\n\t"
    "v_mul_f32 v202, v202, v245\n\t"
    "v_mul_f32 v203, v203, v245\n\t"
    "v_cvt_pk_bf16_f32 v195, v198, v199\n\t"
    "v_cvt_pk_bf16_f32 v201, v202, v203\n\t"
    "v_mul_f32 v204, v204, v245\n\t"
    "v_mul_f32 v205, v205, v245\n\t"
    "v_mfma_f32_16x16x32_bf16 v[76:79], a[240:243], v[32:35], v[76:79]\n\t"
    "v_cvt_pk_bf16_f32 v202, v204, v205\n\t"
    "v_mul_f32 v206, v206, v245\n\t"
    "v_mul_f32 v207, v207, v245\n\t"
    "v_cvt_pk_bf16_f32 v203, v206, v207\n\t"
    "v_mfma_f32_16x16x32_bf16 v[96:99], v[160:163], v[192:195], v[96:99]\n\t"
    "v_cvt_pk_bf16_f32 v167, v230, v231\n\t"
    "v_cvt_pk_bf16_f32 v170, v232, v233\n\t"
    "v_mfma_f32_16x16x32_bf16 v[64:67], v[160:163], v[200:203], v[64:67]\n\t"
    "v_cvt_pk_bf16_f32 v171, v234, v235\n\t"
    "v_cvt_pk_bf16_f32 v174, v236, v237\n\t"
    "v_mfma_f32_16x16x32_bf16 v[68:71], v[164:167], v[200:203], v[68:71]\n\t"
    "v_cvt_pk_bf16_f32 v175, v238, v239\n\t"
    "v_mfma_f32_16x16x32_bf16 v[100:103], v[164:167], v[192:195], v[100:103]\n\t"
    "v_mfma_f32_16x16x32_bf16 v[104:107], v[168:171], v[192:195], v[104:107]\n\t"
    "v_cvt_pk_bf16_f32 v96, v96, v97\n\t"
    "v_cvt_pk_bf16_f32 v97, v98, v99\n\t"
    "v_mfma_f32_16x16x32_bf16 v[72:75], v[168:171], v[200:203], v[72:75]\n\t"
    "v_cvt_pk_bf16_f32 v64, v64, v65\n\t"
    "v_cvt_pk_bf16_f32 v65, v66, v67\n\t"
    "v_mfma_f32_16x16x32_bf16 v[76:79], v[172:175], v[200:203], v[76:79]\n\t"
    "v_cvt_pk_bf16_f32 v66, v68, v69\n\t"
    "v_cvt_pk_bf16_f32 v67, v70, v71\n\t"
    "v_mfma_f32_16x16x32_bf16 v[108:111], v[172:175], v[192:195], v[108:111]\n\t"
    "v_pk_max_i16 v64, v64, 0\n\t"
    "v_pk_max_i16 v65, v65, 0\n\t"
    "v_cvt_pk_bf16_f32 v104, v104, v105\n\t"
    "v_cvt_pk_bf16_f32 v105, v106, v107\n\t"
    "v_pk_max_i16 v66, v66, 0\n\t"
    "v_pk_max_i16 v67, v67, 0\n\t"
    "v_cvt_pk_bf16_f32 v72, v72, v73\n\t"
    "v_cvt_pk_bf16_f32 v73, v74, v75\n\t"
    "v_pk_max_i16 v72, v72, 0\n\t"
    "v_pk_max_i16 v73, v73, 0\n\t"
    "v_cvt_pk_bf16_f32 v74, v76, v77\n\t"
    "v_cvt_pk_bf16_f32 v75, v78, v79\n\t"
    "v_pk_max_i16 v74, v74, 0\n\t"
    "v_pk_max_i16 v75, v75, 0\n\t"
    "v_cvt_pk_bf16_f32 v98, v100, v101\n\t"
    "v_cvt_pk_bf16_f32 v106, v108, v109\n\t"
    "v_cvt_pk_bf16_f32 v99, v102, v103\n\t"
    "v_cvt_pk_bf16_f32 v107, v110, v111\n\t"
    "v_pk_max_i16 v96, v96, 0\n\t"
    "v_pk_max_i16 v97, v97, 0\n\t"
    "v_pk_max_i16 v98, v98, 0\n\t"
    "v_pk_max_i16 v99, v99, 0\n\t"
    "v_pk_max_i16 v104, v104, 0\n\t"
    "v_pk_max_i16 v105, v105, 0\n\t"
    "v_pk_max_i16 v106, v106, 0\n\t"
    "v_pk_max_i16 v107, v107, 0\n\t"
    ".Ls4_%=:\n\t"
    "s_cbranch_execz .Lret%=\n\t"
    "v_cndmask_b32_dpp v76, v72, v64, vcc row_ror:8 row_mask:0xf bank_mask:0xf\n\t"
    "v_cndmask_b32_dpp v77, v73, v65, vcc row_ror:8 row_mask:0xf bank_mask:0xf\n\t"
    "v_cndmask_b32_dpp v78, v74, v66, vcc row_ror:8 row_mask:0xf bank_mask:0xf\n\t"
    "v_cndmask_b32_dpp v79, v75, v67, vcc row_ror:8 row_mask:0xf bank_mask:0xf\n\t"
    "v_mov_b32_dpp v72, v64 row_ror:8 row_mask:0xf bank_mask:0x3\n\t"
    "v_mov_b32_dpp v73, v65 row_ror:8 row_mask:0xf bank_mask:0x3\n\t"
    "v_mov_b32_dpp v74, v66 row_ror:8 row_mask:0xf bank_mask:0x3\n\t"
    "buffer_store_dwordx4 v[76:79], v12, s[24:27], s72 offen offset:0" STPOL "\n\t"
    "v_mov_b32_dpp v75, v67 row_ror:8 row_mask:0xf bank_mask:0x3\n\t"
    "v_cndmask_b32_dpp v108, v104, v96, vcc row_ror:8 row_mask:0xf bank_mask:0xf\n\t"
    "v_cndmask_b32_dpp v109, v105, v97, vcc row_ror:8 row_mask:0xf bank_mask:0xf\n\t"
    "v_cndmask_b32_dpp v110, v106, v98, vcc row_ror:8 row_mask:0xf bank_mask:0xf\n\t"
    "buffer_store_dwordx4 v[72:75], v12, s[24:27], s72 offen offset:2048" STPOL "\n\t"
    "v_cndmask_b32_dpp v111, v107, v99, vcc row_ror:8 row_mask:0xf bank_mask:0xf\n\t"
    "v_mov_b32_dpp v104, v96 row_ror:8 row_mask:0xf bank_mask:0x3\n\t"
    "v_mov_b32_dpp v105, v97 row_ror:8 row_mask:0xf bank_mask:0x3\n\t"
    "v_mov_b32_dpp v106, v98 row_ror:8 row_mask:0xf bank_mask:0x3\n\t"
    "buffer_store_dwordx4 v[108:111], v17, s[24:27], s70 offen offset:0" STPOL "\n\t"
    "v_mov_b32_dpp v107, v99 row_ror:8 row_mask:0xf bank_mask:0x3\n\t"
    "buffer_store_dwordx4 v[104:107], v12, s[24:27], s71 offen offset:2048" STPOL "\n\t"
    :
    : "s"(ka), "s"(bid), "v"(tid), "s"(lb)
    : "v1", "v2", "v3", "v4", "v5", "v6", "v7", "v8", "v9", "v10", "v11", "v12", "v13", "v14", "v15", "v16", "v17", "v18", "v19", "v20", "v21", "v22", "v23", "v24", "v25", "v26", "v27", "v28", "v29", "v30", "v31", "v32", "v33", "v34", "v35", "v36", "v37", "v38", "v39", "v40", "v41", "v42", "v43", "v44", "v45", "v46", "v47", "v48", "v49", "v50", "v51", "v52", "v53", "v54", "v55", "v56", "v57", "v58", "v59", "v60", "v61", "v62", "v63", "v64", "v65", "v66", "v67", "v68", "v69", "v70", "v71", "v72", "v73", "v74", "v75", "v76", "v77", "v78", "v79", "v80", "v81", "v82", "v83", "v84", "v85", "v86", "v87", "v88", "v89", "v90", "v91", "v92", "v93", "v94", "v95", "v96", "v97", "v98", "v99", "v100", "v101", "v102", "v103", "v104", "v105", "v106", "v107", "v108", "v109", "v110", "v111", "v112", "v113", "v114", "v115", "v116", "v117", "v118", "v119", "v120", "v121", "v122", "v123", "v124", "v125", "v126", "v127", "v128", "v129", "v130", "v131", "v132", "v133", "v134", "v135", "v136", "v137", "v138", "v139", "v140", "v141", "v142", "v143", "v144", "v145", "v146", "v147", "v148", "v149", "v150", "v151", "v152", "v153", "v154", "v155", "v156", "v157", "v158", "v159", "v160", "v161", "v162", "v163", "v164", "v165", "v166", "v167", "v168", "v169", "v170", "v171", "v172", "v173", "v174", "v175", "v176", "v177", "v178", "v179", "v180", "v181", "v182", "v183", "v184", "v185", "v186", "v187", "v188", "v189", "v190", "v191", "v192", "v193", "v194", "v195", "v196", "v197", "v198", "v199", "v200", "v201", "v202", "v203", "v204", "v205", "v206", "v207", "v208", "v209", "v210", "v211", "v212", "v213", "v214", "v215", "v216", "v217", "v218", "v219", "v220", "v221", "v222", "v223", "v224", "v225", "v226", "v227", "v228", "v229", "v230", "v231", "v232", "v233", "v234", "v235", "v236", "v237", "v238", "v239", "v240", "v241", "v242", "v243", "v244", "v245", "v246", "v247", "v248", "v249", "v250", "v251", "v252", "v253", "v254", "v255", "a0", "a1", "a2", "a3", "a4", "a5", "a6", "a7", "a8", "a9", "a10", "a11", "a12", "a13", "a14", "a15", "a16", "a17", "a18", "a19", "a20", "a21", "a22", "a23", "a24", "a25", "a26", "a27", "a28", "a29", "a30", "a31", "a32", "a33", "a34", "a35", "a36", "a37", "a38", "a39", "a40", "a41", "a42", "a43", "a44", "a45", "a46", "a47", "a48", "a49", "a50", "a51", "a52", "a53", "a54", "a55", "a56", "a57", "a58", "a59", "a60", "a61", "a62", "a63", "a64", "a65", "a66", "a67", "a68", "a69", "a70", "a71", "a72", "a73", "a74", "a75", "a76", "a77", "a78", "a79", "a80", "a81", "a82", "a83", "a84", "a85", "a86", "a87", "a88", "a89", "a90", "a91", "a92", "a93", "a94", "a95", "a96", "a97", "a98", "a99", "a100", "a101", "a102", "a103", "a104", "a105", "a106", "a107", "a108", "a109", "a110", "a111", "a112", "a113", "a114", "a115", "a116", "a117", "a118", "a119", "a120", "a121", "a122", "a123", "a124", "a125", "a126", "a127", "a128", "a129", "a130", "a131", "a132", "a133", "a134", "a135", "a136", "a137", "a138", "a139", "a140", "a141", "a142", "a143", "a144", "a145", "a146", "a147", "a148", "a149", "a150", "a151", "a152", "a153", "a154", "a155", "a156", "a157", "a158", "a159", "a160", "a161", "a162", "a163", "a164", "a165", "a166", "a167", "a168", "a169", "a170", "a171", "a172", "a173", "a174", "a175", "a176", "a177", "a178", "a179", "a180", "a181", "a182", "a183", "a184", "a185", "a186", "a187", "a188", "a189", "a190", "a191", "a192", "a193", "a194", "a195", "a196", "a197", "a198", "a199", "a200", "a201", "a202", "a203", "a204", "a205", "a206", "a207", "a208", "a209", "a210", "a211", "a212", "a213", "a214", "a215", "a216", "a217", "a218", "a219", "a220", "a221", "a222", "a223", "a224", "a225", "a226", "a227", "a228", "a229", "a230", "a231", "a232", "a233", "a234", "a235", "a236", "a237", "a238", "a239", "a240", "a241", "a242", "a243", "a244", "a245", "a246", "a247", "a248", "a249", "a250", "a251", "a252", "a253", "a254", "a255", "s4", "s5", "s6", "s7", "s8", "s9", "s10", "s11", "s12", "s13", "s14", "s15", "s16", "s17", "s18", "s19", "s20", "s21", "s22", "s23", "s24", "s25", "s26", "s27", "s28", "s29", "s30", "s31", "s33", "s34", "s35", "s36", "s37", "s38", "s39", "s40", "s41", "s42", "s43", "s44", "s45", "s46", "s47", "s48", "s49", "s50", "s51", "s52", "s53", "s54", "s55", "s56", "s57", "s58", "s59", "s60", "s61", "s62", "s63", "s64", "s65", "s66", "s67", "s68", "s69", "s70", "s71", "s72", "s73", "scc", "vcc", "memory");
}

extern "C" void kernel_launch(void* const* d_in, const int* in_sizes, int n_in, void* d_out, int out_size,
                              void* d_ws, size_t ws_size, hipStream_t stream) {
  const __hip_bfloat16* e = (const __hip_bfloat16*)d_in[0];
  const __hip_bfloat16* wq = (const __hip_bfloat16*)d_in[1];
  const __hip_bfloat16* wk = (const __hip_bfloat16*)d_in[2];
  const __hip_bfloat16* wv = (const __hip_bfloat16*)d_in[3];
  const __hip_bfloat16* wres = (const __hip_bfloat16*)d_in[4];
  __hip_bfloat16* out = (__hip_bfloat16*)d_out;
  const int B = in_sizes[0] / (F_ * D_);
  autoint_kernel<<<256 * ((B + 4095) / 4096), 256, 0, stream>>>(e, wq, wk, wv, wres, out, B
 );
}
```
